```python
import torch

def _softmax(x, axis=-1):
    return torch.softmax(x, dim=axis)
def _gelu(x):
    return torch.nn.functional.gelu(x, approximate="tanh")
def _silu(x):
    return torch.nn.functional.silu(x)
def _sigmoid(x):
    return torch.sigmoid(x)
def _layernorm(x, axis=-1, eps=1e-5):
    m = x.mean(dim=axis, keepdim=True)
    v = x.var(dim=axis, keepdim=True, unbiased=False)
    return (x - m) / torch.sqrt(v + eps)
def _scatter_add(tgt, idx, src, axis=0):
    return tgt.clone().index_add_(axis, idx, src)
def _segment_sum(src, seg, n):
    out = torch.zeros((n,) + src.shape[1:], dtype=src.dtype)
    return out.index_add_(0, seg, src)
def _pad(x, pw):
    flat = tuple(v for pair in reversed(pw) for v in pair)
    return torch.nn.functional.pad(x, flat)

kernel_name = 'standalone__seq2seq_rnn_t_joint__speech'

def setup_inputs():

    _w = (lambda a: (lambda t: t.to(torch.bfloat16) if t.is_floating_point() else t)(torch.as_tensor(a)))
    B, T, D, K = 2, 16000, 256, 3
    U = 8
    sc = (1.0 / (K * D)) ** 0.5
    sd = (1.0 / D) ** 0.5
    return {
        "x": _w((lambda *s: torch.randn(*s, dtype=torch.bfloat16) * 0.2)(B, T, D) * 0.5),
        "w1": _w((lambda *s: torch.randn(*s, dtype=torch.bfloat16) * 3.2)(K, D, D) * sc),
        "w2": _w((lambda *s: torch.randn(*s, dtype=torch.bfloat16) * 3.2)(K, D, D) * sc),
        "g": _w((lambda *s: torch.randn(*s, dtype=torch.bfloat16) * 3.2)(B, U, D) * 0.5),
        "w_f": _w((lambda *s: torch.randn(*s, dtype=torch.bfloat16) * 3.2)(D, D) * sd),
        "w_g": _w((lambda *s: torch.randn(*s, dtype=torch.bfloat16) * 3.2)(D, D) * sd),
        "w_o": _w((lambda *s: torch.randn(*s, dtype=torch.bfloat16) * 3.2)(D, D) * sd),
    }

def reference(x, w1, w2, g, w_f, w_g, w_o):
    B, T, D = x.shape
    K = w1.shape[0]
    pad = K // 2
    xp = _pad(x, ((0,0),(pad,pad),(0,0)))
    h = torch.zeros_like(x)
    for i in range(K):
        h = h + torch.einsum("btc,cd->btd", xp[:, i:i+T, :], w1[i])
    h = _gelu(h)[:, ::2, :]
    T1 = h.shape[1]
    hp = _pad(h, ((0,0),(pad,pad),(0,0)))
    f = torch.zeros_like(h)
    for i in range(K):
        f = f + torch.einsum("btc,cd->btd", hp[:, i:i+T1, :], w2[i])
    f = _gelu(f)[:, ::2, :]
    fe = torch.einsum("btd,de->bte", f, w_f)
    ge = torch.einsum("bud,de->bue", g, w_g)
    j = torch.tanh(fe[:, :, None, :] + ge[:, None, :, :])
    return torch.einsum("btud,dv->btuv", j, w_o)
```

```cpp
#include <hip/hip_runtime.h>
#include <hip/hip_bf16.h>
typedef __hip_bfloat16 bf16;
#define MIMIC 0
__device__ __forceinline__ float b2f(bf16 v){ return __bfloat162float(v); }
__device__ __forceinline__ float rb(float v){ return __bfloat162float(__float2bfloat16(v)); }
__device__ __forceinline__ float gelu(float x){
  float u = 0.7978845608028654f*(x+0.044715f*x*x*x);
  return 0.5f*x*(1.f+tanhf(u));
}
__global__ void conv_k(const bf16* in, const bf16* w, bf16* out, int Tin, int Tout){
  int row = blockIdx.x; int b = row / Tout, t = row % Tout; int d = threadIdx.x;
  __shared__ float xs[3][256];
  for(int i=0;i<3;i++){ int tt = 2*t+i-1; xs[i][d] = (tt>=0 && tt<Tin) ? b2f(in[((size_t)b*Tin+tt)*256+d]) : 0.f; }
  __syncthreads();
  float h = 0.f;
  for(int i=0;i<3;i++){
    float a=0.f;
    for(int c=0;c<256;c++) a += xs[i][c]*b2f(w[(i*256+c)*256+d]);
    if (MIMIC) h = rb(h + rb(a)); else h += a;
  }
  out[(size_t)row*256+d] = __float2bfloat16(gelu(h));
}
__global__ void mm_k(const bf16* in, const bf16* w, float* out){
  int row = blockIdx.x; int d = threadIdx.x;
  __shared__ float xs[256];
  xs[d] = b2f(in[(size_t)row*256+d]);
  __syncthreads();
  float a=0.f;
  for(int c=0;c<256;c++) a += xs[c]*b2f(w[c*256+d]);
  out[(size_t)row*256+d] = MIMIC ? rb(a) : a;
}
__global__ void joint_k(const float* fe, const float* ge, const bf16* wo, bf16* out){
  int row = blockIdx.x;
  int b = row / 4000; int d = threadIdx.x;
  __shared__ float js[8][256];
  for(int u=0;u<8;u++){ float s = fe[(size_t)row*256+d] + ge[(b*8+u)*256+d]; if (MIMIC) s = rb(s); js[u][d] = rb(tanhf(s)); }
  __syncthreads();
  float a[8]={0,0,0,0,0,0,0,0};
  for(int c=0;c<256;c++){ float w = b2f(wo[c*256+d]); for(int u=0;u<8;u++) a[u] += js[u][c]*w; }
  for(int u=0;u<8;u++) out[((size_t)row*8+u)*256+d] = __float2bfloat16(a[u]);
}
extern "C" void kernel_launch(void* const* d_in, const int* in_sizes, int n_in,
                              void* d_out, int out_size, void* d_ws, size_t ws_size,
                              hipStream_t stream) {
  const bf16* x=(const bf16*)d_in[0]; const bf16* w1=(const bf16*)d_in[1]; const bf16* w2=(const bf16*)d_in[2];
  const bf16* g=(const bf16*)d_in[3]; const bf16* wf=(const bf16*)d_in[4]; const bf16* wg=(const bf16*)d_in[5]; const bf16* wo=(const bf16*)d_in[6];
  char* ws=(char*)d_ws;
  bf16* h=(bf16*)ws; bf16* f=(bf16*)(ws+(16u<<20)); float* fe=(float*)(ws+(32u<<20)); float* ge=(float*)(ws+(48u<<20));
  conv_k<<<16000,256,0,stream>>>(x,w1,h,16000,8000);
  conv_k<<<8000,256,0,stream>>>(h,w2,f,8000,4000);
  mm_k<<<8000,256,0,stream>>>(f,wf,fe);
  mm_k<<<16,256,0,stream>>>(g,wg,ge);
  joint_k<<<8000,256,0,stream>>>(fe,ge,wo,(bf16*)d_out);
}
```

```cpp
#define STORE_AUX 18
#define LPRIO 3
#define TOUCH 1
#define OTOUCH 0
#define HELPERS 0
#define HZERO 1
#define ZBASE 71680
#define CODE_UNITS 7
#define TSCR2 71424
#include <hip/hip_runtime.h>
#include <hip/hip_bf16.h>
typedef short s16x4 __attribute__((ext_vector_type(4)));
typedef short s16x8 __attribute__((ext_vector_type(8)));
typedef __bf16 bf16x8 __attribute__((ext_vector_type(8)));
typedef __bf16 bf16x4 __attribute__((ext_vector_type(4)));
typedef __bf16 bf16x2 __attribute__((ext_vector_type(2)));
typedef float f32x2 __attribute__((ext_vector_type(2)));
typedef float f32x4 __attribute__((ext_vector_type(4)));
typedef float f32x16 __attribute__((ext_vector_type(16)));
typedef int i32x4 __attribute__((ext_vector_type(4)));
typedef unsigned u32x2 __attribute__((ext_vector_type(2)));
typedef unsigned u32x4 __attribute__((ext_vector_type(4)));
#define LAS __attribute__((address_space(3)))
#define GAS __attribute__((address_space(1)))
typedef LAS s16x4* lds_s16x4_p;
typedef LAS s16x8* lds_s16x8_p;
typedef LAS u32x4* lds_u32x4_p;
typedef LAS f32x4* lds_f32x4_p;
typedef LAS unsigned short* lds_u16_p;
typedef LAS unsigned* lds_u32_p;
typedef LAS char* lds_char_p;
typedef GAS unsigned gu32; typedef GAS unsigned long long gu64;
#define RLX_AGENT __ATOMIC_RELAXED, __HIP_MEMORY_SCOPE_AGENT
#define DI __device__ __forceinline__
#define SB __builtin_amdgcn_sched_barrier(0)
#define TR(addr) __builtin_amdgcn_ds_read_tr16_b64_v4i16((lds_s16x4_p)(lds + (addr)))
#define RD(addr) (*(lds_s16x8_p)(lds + (addr)))
#define BC(x) __builtin_bit_cast(bf16x8,x)
#define CAT(l,h) BC(__builtin_shufflevector(l,h,0,1,2,3,4,5,6,7))
#define MF16(acc,l,h,b) do{ acc=__builtin_amdgcn_mfma_f32_16x16x32_bf16(CAT(l,h),BC(b),acc,0,0,0); asm volatile("":"+v"(acc)); }while(0)
#define MF32(acc,l,h,b) do{ acc=__builtin_amdgcn_mfma_f32_32x32x16_bf16(CAT(l,h),BC(b),acc,0,0,0); asm volatile("":"+v"(acc)); }while(0)
#define Q4(v,i) __builtin_shufflevector(v,v,4*(i),4*(i)+1,4*(i)+2,4*(i)+3)
#define BAR() do{ asm volatile("":::"memory"); __builtin_amdgcn_s_waitcnt(0xC07F); __builtin_amdgcn_s_barrier(); asm volatile("":::"memory"); SB; }while(0)
#define OSTORE(v,vo,so) do{ SB; __builtin_amdgcn_raw_buffer_store_b128(v,r_out,vo,so,STORE_AUX); SB; asm volatile("s_nop 1"); SB; }while(0)
#define LBAR() asm volatile("s_barrier":::"memory")
#define WAITVM(n) asm volatile("s_waitcnt vmcnt(" #n ")":::"memory")
#define OPQ(x) asm volatile("":"+v"(x))
#define TWO_LOG2E 2.8853900817779268f
#define EXP_LIM 100.0f
#define WS_REQ 16384
#define WS_ACK 20480
#define WS_DONE 24576
DI void dma16(i32x4 rsrc, int voff, int soff, int ldsaddr){
  asm volatile("s_mov_b32 m0, %3\n\ts_nop 0\n\tbuffer_load_dwordx4 %0, %1, %2 offen lds" :: "v"(voff), "s"(rsrc), "s"(soff), "s"(ldsaddr) : "memory");
}
DI void dma16nt(i32x4 rsrc, int voff, int soff, int ldsaddr){
  asm volatile("s_mov_b32 m0, %3\n\ts_nop 0\n\tbuffer_load_dwordx4 %0, %1, %2 offen nt lds" :: "v"(voff), "s"(rsrc), "s"(soff), "s"(ldsaddr) : "memory");
}
#define TOUCH_NT ""
DI void touch(i32x4 rsrc, int voff, int ldsaddr){
  asm volatile("s_mov_b32 m0, %2\n\ts_nop 3\n\tbuffer_load_dword %0, %1, 0 offen" TOUCH_NT " lds" :: "v"(voff), "s"(rsrc), "s"(ldsaddr) : "memory");
}
DI i32x4 mkrsrc(const void* p, int bytes){
  unsigned long long a=(unsigned long long)p; i32x4 r;
  r[0]=__builtin_amdgcn_readfirstlane((int)a); r[1]=__builtin_amdgcn_readfirstlane((int)(a>>32)); r[2]=bytes; r[3]=0x00020000; return r;
}
DI float gelu1f(float x){
  const float c1 = -2.0f*0.7978845608028654f*1.4426950408889634f, c2 = c1*0.044715f;
  return x*__builtin_amdgcn_rcpf(1.0f+__builtin_amdgcn_exp2f(x*(x*x*c2+c1)));
}
struct GeluC { f32x2 c1, c2, one; };
DI GeluC gelu_consts(){
  const float c1 = -2.0f*0.7978845608028654f*1.4426950408889634f, c2 = c1*0.044715f;
  GeluC g; g.c1=(f32x2){c1,c1}; g.c2=(f32x2){c2,c2}; g.one=(f32x2){1.0f,1.0f};
  asm volatile("" : "+v"(g.c1), "+v"(g.c2), "+v"(g.one));
  return g;
}
DI u32x4 gelu8(f32x4 a, f32x4 b, const GeluC& g){
  f32x2 x[4]={{a[0],a[1]},{a[2],a[3]},{b[0],b[1]},{b[2],b[3]}}; f32x2 t[4]; u32x4 o;
  _Pragma("unroll") for(int i=0;i<4;i++) t[i]=x[i]*x[i];
  _Pragma("unroll") for(int i=0;i<4;i++) t[i]=__builtin_elementwise_fma(t[i],g.c2,g.c1);
  _Pragma("unroll") for(int i=0;i<4;i++) t[i]=t[i]*x[i];
  _Pragma("unroll") for(int i=0;i<4;i++){ t[i][0]=__builtin_amdgcn_exp2f(t[i][0]); t[i][1]=__builtin_amdgcn_exp2f(t[i][1]); }
  _Pragma("unroll") for(int i=0;i<4;i++) t[i]=t[i]+g.one;
  _Pragma("unroll") for(int i=0;i<4;i++){ t[i][0]=__builtin_amdgcn_rcpf(t[i][0]); t[i][1]=__builtin_amdgcn_rcpf(t[i][1]); }
  _Pragma("unroll") for(int i=0;i<4;i++){ t[i]=t[i]*x[i]; o[i]=__builtin_bit_cast(unsigned,__builtin_convertvector(t[i],bf16x2)); }
  return o;
}
DI unsigned pk2(float a, float b){ f32x2 pr={a,b}; return __builtin_bit_cast(unsigned,__builtin_convertvector(pr,bf16x2)); }
#define OMOD 1
#define RCPM " mul:2"
#define JCON "1.0"
#define JCONF 1.0f
#define FPMODE() asm volatile("s_setreg_imm32_b32 hwreg(HW_REG_MODE, 4, 2), 0\n\ts_setreg_imm32_b32 hwreg(HW_REG_MODE, 9, 1), 0\n\ts_nop 1")
#define DPPC(n) " row_newbcast:%c" #n " row_mask:0xf bank_mask:0xf\n\t"
template<int O> DI u32x2 jhalf(float eg, f32x4 r){
  f32x2 d0,d1; u32x2 j; const f32x2 hf={JCONF,JCONF};
  f32x2 a0={r[0],r[1]}, a1={r[2],r[3]};
  asm volatile(
    "v_add_f32_dpp %0, %4, %5" DPPC(9) "v_add_f32_dpp %1, %4, %6" DPPC(10)
    "v_rcp_f32 %0, %0" RCPM "\n\t" "v_add_f32_dpp %2, %4, %7" DPPC(11)
    "v_rcp_f32 %1, %1" RCPM "\n\t" "v_add_f32_dpp %3, %4, %8" DPPC(12)
    "v_rcp_f32 %2, %2" RCPM "\n\t" "v_rcp_f32 %3, %3" RCPM "\n\ts_nop 0"
    : "=&v"(d0[0]),"=&v"(d0[1]),"=&v"(d1[0]),"=&v"(d1[1])
    : "v"(eg), "v"(r[0]),"v"(r[1]),"v"(r[2]),"v"(r[3]), "i"(O),"i"(O+1),"i"(O+2),"i"(O+3));
  d0=hf-a0*d0; d1=hf-a1*d1;
  j[0]=pk2(d0[0],d0[1]); j[1]=pk2(d1[0],d1[1]);
  asm volatile("" : "+v"(j));
  return j;
}
template<int O> DI u32x2 jhalfs(float eg, f32x4 r){
  float d0,d1,d2,d3; u32x2 j;
  asm volatile(
    "v_add_f32_dpp %2, %6, %7" DPPC(11) "v_add_f32_dpp %3, %6, %8" DPPC(12)
    "v_rcp_f32 %2, %2" RCPM "\n\t" "v_add_f32_dpp %4, %6, %9" DPPC(13)
    "v_rcp_f32 %3, %3" RCPM "\n\t" "v_add_f32_dpp %5, %6, %10" DPPC(14)
    "v_rcp_f32 %4, %4" RCPM "\n\t" "v_fma_f32 %2, -%7, %2, " JCON "\n\t"
    "v_rcp_f32 %5, %5" RCPM "\n\t" "v_fma_f32 %3, -%8, %3, " JCON "\n\t"
    "v_fma_f32 %4, -%9, %4, " JCON "\n\t" "v_cvt_pk_bf16_f32 %0, %2, %3\n\t"
    "v_fma_f32 %5, -%10, %5, " JCON "\n\t" "v_cvt_pk_bf16_f32 %1, %4, %5\n\ts_nop 1"
    : "=&v"(j[0]), "=&v"(j[1]), "=&v"(d0),"=&v"(d1),"=&v"(d2),"=&v"(d3)
    : "v"(eg), "v"(r[0]),"v"(r[1]),"v"(r[2]),"v"(r[3]), "i"(O),"i"(O+1),"i"(O+2),"i"(O+3));
  return j;
}
template<int O> DI f32x4 jrcp(float eg, f32x4 r){
  f32x4 d;
  asm volatile(
    "v_add_f32_dpp %0, %4, %5" DPPC(9) "v_add_f32_dpp %1, %4, %6" DPPC(10)
    "v_rcp_f32 %0, %0" RCPM "\n\t" "v_add_f32_dpp %2, %4, %7" DPPC(11)
    "v_rcp_f32 %1, %1" RCPM "\n\t" "v_add_f32_dpp %3, %4, %8" DPPC(12)
    "v_rcp_f32 %2, %2" RCPM "\n\t" "v_rcp_f32 %3, %3" RCPM "\n\ts_nop 0"
    : "=&v"(d[0]),"=&v"(d[1]),"=&v"(d[2]),"=&v"(d[3])
    : "v"(eg), "v"(r[0]),"v"(r[1]),"v"(r[2]),"v"(r[3]), "i"(O),"i"(O+1),"i"(O+2),"i"(O+3));
  return d;
}
DI u32x2 jfin(f32x4 r, f32x4 d){
  u32x2 j;
  asm volatile("v_fma_f32 %2, -%6, %2, " JCON "\n\tv_fma_f32 %3, -%7, %3, " JCON "\n\tv_fma_f32 %4, -%8, %4, " JCON "\n\tv_cvt_pk_bf16_f32 %0, %2, %3\n\tv_fma_f32 %5, -%9, %5, " JCON "\n\tv_cvt_pk_bf16_f32 %1, %4, %5\n\ts_nop 1"
    : "=&v"(j[0]), "=&v"(j[1]), "+v"(d[0]),"+v"(d[1]),"+v"(d[2]),"+v"(d[3]) : "v"(r[0]),"v"(r[1]),"v"(r[2]),"v"(r[3]));
  return j;
}
#define SCALEV (OMOD?2:1)
DI unsigned pk2x2(float a, float b){
#if SCALEV==0
  f32x2 pr={a,b}; pr=pr+pr; return __builtin_bit_cast(unsigned,__builtin_convertvector(pr,bf16x2));
#elif SCALEV==1
  return pk2(__builtin_amdgcn_ldexpf(a,1),__builtin_amdgcn_ldexpf(b,1));
#else
  return pk2(a,b);
#endif
}
template<unsigned MASK, int PERM> DI u32x4 qsel(u32x4 a, u32x4 b){
  u32x4 d;
  if(PERM==0xB1) asm volatile("s_mov_b32 vcc_lo, %12\n\ts_mov_b32 vcc_hi, %12\n\t"
    "v_cndmask_b32_dpp %0, %4, %8, vcc quad_perm:[1,0,3,2] row_mask:0xf bank_mask:0xf\n\t" "v_cndmask_b32_dpp %1, %5, %9, vcc quad_perm:[1,0,3,2] row_mask:0xf bank_mask:0xf\n\t"
    "v_cndmask_b32_dpp %2, %6, %10, vcc quad_perm:[1,0,3,2] row_mask:0xf bank_mask:0xf\n\t" "v_cndmask_b32_dpp %3, %7, %11, vcc quad_perm:[1,0,3,2] row_mask:0xf bank_mask:0xf"
    : "=&v"(d[0]),"=&v"(d[1]),"=&v"(d[2]),"=&v"(d[3]) : "v"(a[0]),"v"(a[1]),"v"(a[2]),"v"(a[3]),"v"(b[0]),"v"(b[1]),"v"(b[2]),"v"(b[3]), "i"(MASK) : "vcc");
  else asm volatile("s_mov_b32 vcc_lo, %12\n\ts_mov_b32 vcc_hi, %12\n\t"
    "v_cndmask_b32_dpp %0, %4, %8, vcc quad_perm:[2,3,0,1] row_mask:0xf bank_mask:0xf\n\t" "v_cndmask_b32_dpp %1, %5, %9, vcc quad_perm:[2,3,0,1] row_mask:0xf bank_mask:0xf\n\t"
    "v_cndmask_b32_dpp %2, %6, %10, vcc quad_perm:[2,3,0,1] row_mask:0xf bank_mask:0xf\n\t" "v_cndmask_b32_dpp %3, %7, %11, vcc quad_perm:[2,3,0,1] row_mask:0xf bank_mask:0xf"
    : "=&v"(d[0]),"=&v"(d[1]),"=&v"(d[2]),"=&v"(d[3]) : "v"(a[0]),"v"(a[1]),"v"(a[2]),"v"(a[3]),"v"(b[0]),"v"(b[1]),"v"(b[2]),"v"(b[3]), "i"(MASK) : "vcc");
  return d;
}
template<int BM> DI f32x4 hbc(f32x4 o, f32x4 v){
  _Pragma("unroll") for(int r=0;r<4;r++) o[r]=__int_as_float(__builtin_amdgcn_update_dpp(__float_as_int(o[r]),__float_as_int(v[r]),0x150,0xf,BM,false));
  return o;
}
DI float max3abs(float m, float a, float b){ float r; asm("v_max3_f32 %0, %1, |%2|, |%3|" : "=v"(r) : "v"(m), "v"(a), "v"(b)); return r; }
template<int I> DI float slow1(float eg, float r){
  float g=__int_as_float(__builtin_amdgcn_update_dpp(0,__float_as_int(eg),0x150+I,0xf,0xf,false));
  float z=r-g; float c=fminf(fmaxf(z,-126.f),126.f);
  return __builtin_amdgcn_exp2f((z!=z)?z:c);
}
template<int O> DI void slow_fix(float eg, f32x4& r){
  r[0]=slow1<O>(eg,r[0]); r[1]=slow1<O+1>(eg,r[1]); r[2]=slow1<O+2>(eg,r[2]); r[3]=slow1<O+3>(eg,r[3]);
}
DI unsigned hs_finish(unsigned long long a, unsigned r, gu64* ack, gu32* done, int lane){
  unsigned spins=0;
  while(__builtin_amdgcn_readfirstlane((unsigned)a)!=__builtin_amdgcn_readfirstlane(r)){
    if(++spins>4096u) return 2u;
    __builtin_amdgcn_s_sleep(8);
    a=__hip_atomic_load(ack, RLX_AGENT);
  }
  if(lane==0) __hip_atomic_store(done,(unsigned)(a>>32), RLX_AGENT);
  return 1u;
}
DI void ge_local(const char* g2, const char* wg, int lane, f32x4& o0, f32x4& o1){
  f32x4 a0={0.f,0.f,0.f,0.f}, a1={0.f,0.f,0.f,0.f};
  const unsigned short* gp=(const unsigned short*)g2;
  _Pragma("unroll 4") for(int d=0;d<256;d++){
    u32x2 wv=*(const u32x2*)(wg+d*512+lane*8);
    f32x4 wf4={__uint_as_float(wv[0]<<16),__uint_as_float(wv[0]&0xffff0000u),__uint_as_float(wv[1]<<16),__uint_as_float(wv[1]&0xffff0000u)};
    float g0=__uint_as_float((unsigned)gp[d]<<16), g1=__uint_as_float((unsigned)gp[256+d]<<16);
    a0+=wf4*g0; a1+=wf4*g1;
  }
  o0=a0*TWO_LOG2E; o1=a1*TWO_LOG2E;
}
DI void producer(lds_char_p lds, const char* gin, const char* wg, char* ws, int wid, int lane){
  const int tid=wid*64+lane;
  unsigned nonce=0;
  if(tid<250) nonce=__hip_atomic_load((gu32*)(ws+WS_DONE)+tid, RLX_AGENT)+1u;
  { i32x4 r_wg=mkrsrc(wg,256*512);
    const int rp=lane>>3, pos=lane&7, swz=(pos ^ (((rp>>1)&3)<<1))*16;
    int voffW1=(8*(rp>>2)+(rp&3))*512+swz;
    _Pragma("unroll") for(int pi=0;pi<16;pi++){ const int sl=pi>>2, j=pi&3; const int roff=(j<2?16*j:16*(j-2)+4)*512;
      dma16(r_wg, voffW1, wid*16384+roff+sl*128, wid*16384+sl*4096+j*1024); }
  }
  const int col=lane&15, q=lane>>4, a_=(lane&15)>>2, p_=lane&3;
  s16x8 B[8];
  if(wid<4){ _Pragma("unroll") for(int k=0;k<8;k++) B[k]=*(const s16x8*)(gin+col*512+k*64+q*16); }
  WAITVM(0);
  BAR();
  if(wid<4){
    const int arow=4*q+a_, ag=(arow>>1)&3;
    auto r_ws=__builtin_amdgcn_make_buffer_rsrc(ws, 0, 16384, 0x00020000);
    _Pragma("unroll") for(int i=0;i<4;i++){
      int ab=wid*4096+arow*128+((i^ag)*32)+8*p_;
      f32x4 acc={0.f,0.f,0.f,0.f};
      _Pragma("unroll") for(int k=0;k<8;k++){ s16x4 l=TR(ab+k*16384), h=TR(ab+k*16384+2048); MF16(acc,l,h,B[k]); }
      acc=acc*TWO_LOG2E;
      __builtin_amdgcn_raw_buffer_store_b128(__builtin_bit_cast(u32x4,acc), r_ws, col*1024+(wid*64+i*16+q*4)*4, 0, 16);
    }
  }
  WAITVM(0);
  BAR();
  if(wid<4){
    const bool act=tid<250; const int c=act?tid:0;
    unsigned last=0; bool first=true, fin=!act;
    for(unsigned spins=0; spins<20000u; ++spins){
      unsigned rq=__hip_atomic_load((gu32*)(ws+WS_REQ)+c, RLX_AGENT);
      unsigned dn=__hip_atomic_load((gu32*)(ws+WS_DONE)+c, RLX_AGENT);
      if(act && (first || rq!=last)) __hip_atomic_store((gu64*)(ws+WS_ACK)+c, ((unsigned long long)nonce<<32)|rq, RLX_AGENT);
      last=rq; first=false;
      fin = fin || (dn==nonce);
      if(__all(fin)) break;
      __builtin_amdgcn_s_sleep(4);
    }
  }
}

__global__ __launch_bounds__(512,2) void fused_kernel(const char* x, const char* w1, const char* w2, const char* gin, const char* wf, const char* wg, const char* wo, char* outp, char* ws){
  const lds_char_p lds=(lds_char_p)0;
  const int wid=__builtin_amdgcn_readfirstlane(threadIdx.x>>6), lane=threadIdx.x&63;
  const int blk=blockIdx.x;
  const char* code0; int code_sz; asm volatile("s_getpc_b64 %0\n.Lpc0:\n\ts_mov_b32 %1, .Lend1-.Lpc0\n\ts_max_i32 %1, %1, .Lend2-.Lpc0" : "=s"(code0), "=s"(code_sz) :: "scc");
  if(blk==0){ producer(lds,gin,wg,ws,wid,lane); return; }
  const int lam=blk&7, sig=blk>>3;
  const int tile=(lam==0?-1:(lam<3?32*lam-1:31*lam+2))+sig;
  const int b=tile>=125?1:0, t0=(tile-125*b)*32;
  f32x4 acc[4][5];

  if(__builtin_expect(wid>=4,1)){
    const int s_l=wid-4;
    __builtin_amdgcn_s_setprio(LPRIO);
    i32x4 r_x=mkrsrc(x + (size_t)b*16000*512, 16000*512), r_w1=mkrsrc(w1,3*256*512), r_w2=mkrsrc(w2,3*256*512), r_wf=mkrsrc(wf,256*512), r_wo=mkrsrc(wo,256*512);
    const int un=lane>>2;
    int voffX=(un&7)*2048 + (un>>3)*64 + (((lane&3) ^ (((un>>2)&1)<<1))*16);
    int voffHX=((lane>>2)&3)*512 + ((s_l&1)*4+(lane>>4))*64 + (lane&3)*16;
    if(t0==0) voffHX=0x7ffffff0;
    const int s_hxoff = (t0==0)?0:(4*t0-4)*512;
    const int s_xoff=(4*t0+s_l)*512;
    const int rp=lane>>3, pos=lane&7, swz=(pos ^ (((rp>>1)&3)<<1))*16;
    int voffW1=(8*(rp>>2)+(rp&3))*512+swz, voffW2=rp*512+((pos ^ (((rp>>1)&1)<<2))*16), voffWo=rp*512+((pos^(((rp>>1)&1)<<1))*16);
    f32x4 ge0, ge1;
    const char* gesrc = ws + (b*8+2*s_l)*1024 + lane*16;
    #define GE_LOAD() asm volatile("global_load_dwordx4 %0, %2, off sc1\n\tglobal_load_dwordx4 %1, %2, off offset:1024 sc1" : "=&v"(ge0), "=&v"(ge1) : "v"(gesrc) : "memory")
    #define GE_WRITE() do{ asm volatile("" : "+v"(ge0), "+v"(ge1)); unsigned gf=__builtin_amdgcn_readfirstlane(gflag); \
        if(gf!=1u){ ge_local(gin+(b*8+2*s_l)*512, wg, lane, ge0, ge1); WAITVM(0); } float m_=0.f; _Pragma("unroll") for(int r=0;r<4;r++) m_=max3abs(m_,ge0[r],ge1[r]); \
        *(lds_f32x4_p)(lds+73728+s_l*2048+lane*16)=ge0; *(lds_f32x4_p)(lds+73728+s_l*2048+1024+lane*16)=ge1; if(m_>EXP_LIM) *(lds_u32_p)(lds+69632)=1u; }while(0)
    dma16nt(r_x, voffHX, s_hxoff, (s_l&1)*1024 + 65536);
    dma16nt(r_x, voffX, s_xoff + 0, s_l*4096 + 0);
    dma16nt(r_x, voffX, s_xoff + 16384, s_l*4096 + 1024);
    dma16nt(r_x, voffX, s_xoff + 32768, s_l*4096 + 2048);
    dma16nt(r_x, voffX, s_xoff + 49152, s_l*4096 + 3072);
    dma16(r_w1, voffW1, s_l*128 + 0, s_l*4096 + 81920);
    dma16(r_w1, voffW1, s_l*128 + 8192, s_l*4096 + 82944);
    dma16(r_w1, voffW1, s_l*128 + 2048, s_l*4096 + 83968);
    dma16(r_w1, voffW1, s_l*128 + 10240, s_l*4096 + 84992);
        dma16(r_w1, voffW1, s_l*128 + 262144, s_l*4096 + 98304);
    dma16(r_w1, voffW1, s_l*128 + 270336, s_l*4096 + 99328);
    dma16(r_w1, voffW1, s_l*128 + 264192, s_l*4096 + 100352);
    dma16(r_w1, voffW1, s_l*128 + 272384, s_l*4096 + 101376);
    dma16(r_w1, voffW1, s_l*128 + 131072, s_l*4096 + 114688);
    dma16(r_w1, voffW1, s_l*128 + 139264, s_l*4096 + 115712);
    dma16(r_w1, voffW1, s_l*128 + 133120, s_l*4096 + 116736);
    dma16(r_w1, voffW1, s_l*128 + 141312, s_l*4096 + 117760);
        WAITVM(8); LBAR();
    dma16(r_w1, voffW1, s_l*128 + 16384, s_l*4096 + 131072);
    dma16(r_w1, voffW1, s_l*128 + 24576, s_l*4096 + 132096);
    dma16(r_w1, voffW1, s_l*128 + 18432, s_l*4096 + 133120);
    dma16(r_w1, voffW1, s_l*128 + 26624, s_l*4096 + 134144);
    dma16(r_w1, voffW1, s_l*128 + 278528, s_l*4096 + 147456);
    dma16(r_w1, voffW1, s_l*128 + 286720, s_l*4096 + 148480);
    dma16(r_w1, voffW1, s_l*128 + 280576, s_l*4096 + 149504);
    dma16(r_w1, voffW1, s_l*128 + 288768, s_l*4096 + 150528);
        WAITVM(12); LBAR();
    dma16nt(r_x, voffX, s_xoff + 128, s_l*4096 + 16384);
    dma16nt(r_x, voffX, s_xoff + 16512, s_l*4096 + 17408);
    dma16nt(r_x, voffX, s_xoff + 32896, s_l*4096 + 18432);
    dma16nt(r_x, voffX, s_xoff + 49280, s_l*4096 + 19456);
        WAITVM(12); LBAR();
    dma16(r_w1, voffW1, s_l*128 + 147456, s_l*4096 + 81920);
    dma16(r_w1, voffW1, s_l*128 + 155648, s_l*4096 + 82944);
    dma16(r_w1, voffW1, s_l*128 + 149504, s_l*4096 + 83968);
    dma16(r_w1, voffW1, s_l*128 + 157696, s_l*4096 + 84992);
        WAITVM(12); LBAR();
    dma16(r_w1, voffW1, s_l*128 + 32768, s_l*4096 + 98304);
    dma16(r_w1, voffW1, s_l*128 + 40960, s_l*4096 + 99328);
    dma16(r_w1, voffW1, s_l*128 + 34816, s_l*4096 + 100352);
    dma16(r_w1, voffW1, s_l*128 + 43008, s_l*4096 + 101376);
        WAITVM(12); LBAR();
    dma16(r_w1, voffW1, s_l*128 + 294912, s_l*4096 + 114688);
    dma16(r_w1, voffW1, s_l*128 + 303104, s_l*4096 + 115712);
    dma16(r_w1, voffW1, s_l*128 + 296960, s_l*4096 + 116736);
    dma16(r_w1, voffW1, s_l*128 + 305152, s_l*4096 + 117760);
    dma16(r_w1, voffW1, s_l*128 + 163840, s_l*4096 + 131072);
        WAITVM(9); LBAR();
    dma16(r_w1, voffW1, s_l*128 + 172032, s_l*4096 + 132096);
    dma16(r_w1, voffW1, s_l*128 + 165888, s_l*4096 + 133120);
    dma16(r_w1, voffW1, s_l*128 + 174080, s_l*4096 + 134144);
    dma16(r_w1, voffW1, s_l*128 + 49152, s_l*4096 + 147456);
    dma16(r_w1, voffW1, s_l*128 + 57344, s_l*4096 + 148480);
        WAITVM(10); LBAR();
    dma16(r_w1, voffW1, s_l*128 + 51200, s_l*4096 + 149504);
    dma16(r_w1, voffW1, s_l*128 + 59392, s_l*4096 + 150528);
    dma16nt(r_x, voffX, s_xoff + 256, s_l*4096 + 32768);
    dma16nt(r_x, voffX, s_xoff + 16640, s_l*4096 + 33792);
    dma16nt(r_x, voffX, s_xoff + 33024, s_l*4096 + 34816);
        WAITVM(11); LBAR();
    dma16nt(r_x, voffX, s_xoff + 49408, s_l*4096 + 35840);
    dma16(r_w1, voffW1, s_l*128 + 311296, s_l*4096 + 0);
    dma16(r_w1, voffW1, s_l*128 + 319488, s_l*4096 + 1024);
    dma16(r_w1, voffW1, s_l*128 + 313344, s_l*4096 + 2048);
    dma16(r_w1, voffW1, s_l*128 + 321536, s_l*4096 + 3072);
    WAITVM(12); LBAR();
    dma16(r_w1, voffW1, s_l*128 + 180224, s_l*4096 + 81920);
    dma16(r_w1, voffW1, s_l*128 + 188416, s_l*4096 + 82944);
    dma16(r_w1, voffW1, s_l*128 + 182272, s_l*4096 + 83968);
    dma16(r_w1, voffW1, s_l*128 + 190464, s_l*4096 + 84992);
    dma16(r_w1, voffW1, s_l*128 + 65536, s_l*4096 + 98304);
    WAITVM(13); LBAR();
    dma16(r_w1, voffW1, s_l*128 + 73728, s_l*4096 + 99328);
    dma16(r_w1, voffW1, s_l*128 + 67584, s_l*4096 + 100352);
    dma16(r_w1, voffW1, s_l*128 + 75776, s_l*4096 + 101376);
    dma16(r_w1, voffW1, s_l*128 + 327680, s_l*4096 + 114688);
    dma16(r_w1, voffW1, s_l*128 + 335872, s_l*4096 + 115712);
    WAITVM(10); LBAR();
    dma16(r_w1, voffW1, s_l*128 + 329728, s_l*4096 + 116736);
    dma16(r_w1, voffW1, s_l*128 + 337920, s_l*4096 + 117760);
    dma16(r_w1, voffW1, s_l*128 + 196608, s_l*4096 + 131072);
    dma16(r_w1, voffW1, s_l*128 + 204800, s_l*4096 + 132096);
    dma16(r_w1, voffW1, s_l*128 + 198656, s_l*4096 + 133120);
    WAITVM(11); LBAR();
    dma16(r_w1, voffW1, s_l*128 + 206848, s_l*4096 + 134144);
    dma16(r_w1, voffW1, s_l*128 + 81920, s_l*4096 + 147456);
    dma16(r_w1, voffW1, s_l*128 + 90112, s_l*4096 + 148480);
    dma16(r_w1, voffW1, s_l*128 + 83968, s_l*4096 + 149504);
    WAITVM(11); LBAR();
    dma16(r_w1, voffW1, s_l*128 + 92160, s_l*4096 + 150528);
    dma16nt(r_x, voffX, s_xoff + 384, s_l*4096 + 49152);
    dma16nt(r_x, voffX, s_xoff + 16768, s_l*4096 + 50176);
    dma16nt(r_x, voffX, s_xoff + 33152, s_l*4096 + 51200);
    dma16nt(r_x, voffX, s_xoff + 49536, s_l*4096 + 52224);
    WAITVM(12); LBAR();
    dma16(r_w1, voffW1, s_l*128 + 344064, s_l*4096 + 0);
    dma16(r_w1, voffW1, s_l*128 + 352256, s_l*4096 + 1024);
    dma16(r_w1, voffW1, s_l*128 + 346112, s_l*4096 + 2048);
    dma16(r_w1, voffW1, s_l*128 + 354304, s_l*4096 + 3072);
    dma16(r_w1, voffW1, s_l*128 + 212992, s_l*4096 + 16384);
    WAITVM(13); LBAR();
    dma16(r_w1, voffW1, s_l*128 + 221184, s_l*4096 + 17408);
    dma16(r_w1, voffW1, s_l*128 + 215040, s_l*4096 + 18432);
    dma16(r_w1, voffW1, s_l*128 + 223232, s_l*4096 + 19456);
    dma16(r_w1, voffW1, s_l*128 + 98304, s_l*4096 + 81920);
    dma16(r_w1, voffW1, s_l*128 + 106496, s_l*4096 + 82944);
    WAITVM(14); LBAR();
    dma16(r_w1, voffW1, s_l*128 + 100352, s_l*4096 + 83968);
    dma16(r_w1, voffW1, s_l*128 + 108544, s_l*4096 + 84992);
    dma16(r_w1, voffW1, s_l*128 + 360448, s_l*4096 + 98304);
    dma16(r_w1, voffW1, s_l*128 + 368640, s_l*4096 + 99328);
    dma16(r_w1, voffW1, s_l*128 + 362496, s_l*4096 + 100352);
    WAITVM(11); LBAR();
    dma16(r_w1, voffW1, s_l*128 + 370688, s_l*4096 + 101376);
    dma16(r_w1, voffW1, s_l*128 + 229376, s_l*4096 + 114688);
    dma16(r_w1, voffW1, s_l*128 + 237568, s_l*4096 + 115712);
    dma16(r_w1, voffW1, s_l*128 + 231424, s_l*4096 + 116736);
    dma16(r_w1, voffW1, s_l*128 + 239616, s_l*4096 + 117760);
    WAITVM(12); LBAR();
    dma16(r_w1, voffW1, s_l*128 + 114688, s_l*4096 + 131072);
    dma16(r_w1, voffW1, s_l*128 + 122880, s_l*4096 + 132096);
    dma16(r_w1, voffW1, s_l*128 + 116736, s_l*4096 + 133120);
    dma16(r_w1, voffW1, s_l*128 + 124928, s_l*4096 + 134144);
    dma16(r_w1, voffW1, s_l*128 + 376832, s_l*4096 + 147456);
    WAITVM(13); LBAR();
    dma16(r_w1, voffW1, s_l*128 + 385024, s_l*4096 + 148480);
    dma16(r_w1, voffW1, s_l*128 + 378880, s_l*4096 + 149504);
    dma16(r_w1, voffW1, s_l*128 + 387072, s_l*4096 + 150528);
    dma16(r_w1, voffW1, s_l*128 + 245760, s_l*4096 + 0);
    dma16(r_w1, voffW1, s_l*128 + 253952, s_l*4096 + 1024);
    WAITVM(14); LBAR();
    dma16(r_w1, voffW1, s_l*128 + 247808, s_l*4096 + 2048);
    dma16(r_w1, voffW1, s_l*128 + 256000, s_l*4096 + 3072);
    dma16(r_w2, voffW2, s_l*128 + 0, s_l*4096 + 16384);
    dma16(r_w2, voffW2, s_l*128 + 4096, s_l*4096 + 17408);
    dma16(r_w2, voffW2, s_l*128 + 8192, s_l*4096 + 18432);
    WAITVM(15); LBAR();
    dma16(r_w2, voffW2, s_l*128 + 12288, s_l*4096 + 19456);
    dma16(r_w2, voffW2, s_l*128 + 131072, s_l*4096 + 81920);
    dma16(r_w2, voffW2, s_l*128 + 135168, s_l*4096 + 82944);
    dma16(r_w2, voffW2, s_l*128 + 139264, s_l*4096 + 83968);
    dma16(r_w2, voffW2, s_l*128 + 143360, s_l*4096 + 84992);
    WAITVM(16); LBAR();
    dma16(r_w2, voffW2, s_l*128 + 262144, s_l*4096 + 98304);
    dma16(r_w2, voffW2, s_l*128 + 266240, s_l*4096 + 99328);
    dma16(r_w2, voffW2, s_l*128 + 270336, s_l*4096 + 100352);
    dma16(r_w2, voffW2, s_l*128 + 274432, s_l*4096 + 101376);
    dma16(r_w2, voffW2, s_l*128 + 16384, s_l*4096 + 114688);
    WAITVM(17); LBAR();
    dma16(r_w2, voffW2, s_l*128 + 20480, s_l*4096 + 115712);
    dma16(r_w2, voffW2, s_l*128 + 24576, s_l*4096 + 116736);
    dma16(r_w2, voffW2, s_l*128 + 28672, s_l*4096 + 117760);
    dma16(r_w2, voffW2, s_l*128 + 147456, s_l*4096 + 131072);
    dma16(r_w2, voffW2, s_l*128 + 151552, s_l*4096 + 132096);
    WAITVM(18); LBAR();
    dma16(r_w2, voffW2, s_l*128 + 155648, s_l*4096 + 133120);
    dma16(r_w2, voffW2, s_l*128 + 159744, s_l*4096 + 134144);
    dma16(r_w2, voffW2, s_l*128 + 278528, s_l*4096 + 147456);
    dma16(r_w2, voffW2, s_l*128 + 282624, s_l*4096 + 148480);
    dma16(r_w2, voffW2, s_l*128 + 286720, s_l*4096 + 149504);
    WAITVM(15); LBAR();
    dma16(r_w2, voffW2, s_l*128 + 290816, s_l*4096 + 150528);
    dma16(r_w2, voffW2, s_l*128 + 32768, s_l*4096 + 0);
    dma16(r_w2, voffW2, s_l*128 + 36864, s_l*4096 + 1024);
    dma16(r_w2, voffW2, s_l*128 + 40960, s_l*4096 + 2048);
    dma16(r_w2, voffW2, s_l*128 + 45056, s_l*4096 + 3072);
    WAITVM(12); LBAR();
    dma16(r_w2, voffW2, s_l*128 + 163840, s_l*4096 + 16384);
    dma16(r_w2, voffW2, s_l*128 + 167936, s_l*4096 + 17408);
    dma16(r_w2, voffW2, s_l*128 + 172032, s_l*4096 + 18432);
    dma16(r_w2, voffW2, s_l*128 + 176128, s_l*4096 + 19456);
    dma16(r_w2, voffW2, s_l*128 + 294912, s_l*4096 + 81920);
    dma16(r_w2, voffW2, s_l*128 + 299008, s_l*4096 + 82944);
    dma16(r_w2, voffW2, s_l*128 + 303104, s_l*4096 + 83968);
    dma16(r_w2, voffW2, s_l*128 + 307200, s_l*4096 + 84992);
    WAITVM(12); LBAR();
    dma16(r_w2, voffW2, s_l*128 + 49152, s_l*4096 + 98304);
    dma16(r_w2, voffW2, s_l*128 + 53248, s_l*4096 + 99328);
    dma16(r_w2, voffW2, s_l*128 + 57344, s_l*4096 + 100352);
    dma16(r_w2, voffW2, s_l*128 + 61440, s_l*4096 + 101376);
    dma16(r_w2, voffW2, s_l*128 + 180224, s_l*4096 + 114688);
    dma16(r_w2, voffW2, s_l*128 + 184320, s_l*4096 + 115712);
    dma16(r_w2, voffW2, s_l*128 + 188416, s_l*4096 + 116736);
    dma16(r_w2, voffW2, s_l*128 + 192512, s_l*4096 + 117760);
    WAITVM(12); LBAR();
    dma16(r_w2, voffW2, s_l*128 + 311296, s_l*4096 + 131072);
    dma16(r_w2, voffW2, s_l*128 + 315392, s_l*4096 + 132096);
    dma16(r_w2, voffW2, s_l*128 + 319488, s_l*4096 + 133120);
    dma16(r_w2, voffW2, s_l*128 + 323584, s_l*4096 + 134144);
    dma16(r_w2, voffW2, s_l*128 + 65536, s_l*4096 + 147456);
    dma16(r_w2, voffW2, s_l*128 + 69632, s_l*4096 + 148480);
    dma16(r_w2, voffW2, s_l*128 + 73728, s_l*4096 + 149504);
    dma16(r_w2, voffW2, s_l*128 + 77824, s_l*4096 + 150528);
    WAITVM(16); LBAR();
    dma16(r_w2, voffW2, s_l*128 + 196608, s_l*4096 + 0);
    dma16(r_w2, voffW2, s_l*128 + 200704, s_l*4096 + 1024);
    WAITVM(14); LBAR();
    dma16(r_w2, voffW2, s_l*128 + 204800, s_l*4096 + 2048);
    dma16(r_w2, voffW2, s_l*128 + 208896, s_l*4096 + 3072);
    dma16(r_w2, voffW2, s_l*128 + 327680, s_l*4096 + 16384);
    dma16(r_w2, voffW2, s_l*128 + 331776, s_l*4096 + 17408);
    dma16(r_w2, voffW2, s_l*128 + 335872, s_l*4096 + 18432);
    dma16(r_w2, voffW2, s_l*128 + 339968, s_l*4096 + 19456);
    dma16(r_w2, voffW2, s_l*128 + 81920, s_l*4096 + 81920);
    WAITVM(17); LBAR();
    dma16(r_w2, voffW2, s_l*128 + 86016, s_l*4096 + 82944);
    dma16(r_w2, voffW2, s_l*128 + 90112, s_l*4096 + 83968);
    dma16(r_w2, voffW2, s_l*128 + 94208, s_l*4096 + 84992);
    dma16(r_w2, voffW2, s_l*128 + 212992, s_l*4096 + 98304);
    WAITVM(17); LBAR();
    dma16(r_w2, voffW2, s_l*128 + 217088, s_l*4096 + 99328);
    dma16(r_w2, voffW2, s_l*128 + 221184, s_l*4096 + 100352);
    dma16(r_w2, voffW2, s_l*128 + 225280, s_l*4096 + 101376);
    dma16(r_w2, voffW2, s_l*128 + 344064, s_l*4096 + 114688);
    WAITVM(17); LBAR();
    dma16(r_w2, voffW2, s_l*128 + 348160, s_l*4096 + 115712);
    dma16(r_w2, voffW2, s_l*128 + 352256, s_l*4096 + 116736);
    dma16(r_w2, voffW2, s_l*128 + 356352, s_l*4096 + 117760);
    WAITVM(16); LBAR();
    unsigned gflag=*(volatile LAS unsigned*)(lds+69636); GE_LOAD();
    dma16(r_w2, voffW2, s_l*128 + 98304, s_l*4096 + 131072);
    dma16(r_w2, voffW2, s_l*128 + 102400, s_l*4096 + 132096);
    dma16(r_w2, voffW2, s_l*128 + 106496, s_l*4096 + 133120);
    dma16(r_w2, voffW2, s_l*128 + 110592, s_l*4096 + 134144);
    WAITVM(18); LBAR();
    dma16(r_w2, voffW2, s_l*128 + 229376, s_l*4096 + 147456);
    dma16(r_w2, voffW2, s_l*128 + 233472, s_l*4096 + 148480);
    dma16(r_w2, voffW2, s_l*128 + 237568, s_l*4096 + 149504);
    dma16(r_w2, voffW2, s_l*128 + 241664, s_l*4096 + 150528);
    WAITVM(18); LBAR();
    dma16(r_w2, voffW2, s_l*128 + 360448, s_l*4096 + 0);
    dma16(r_w2, voffW2, s_l*128 + 364544, s_l*4096 + 1024);
    dma16(r_w2, voffW2, s_l*128 + 368640, s_l*4096 + 2048);
    dma16(r_w2, voffW2, s_l*128 + 372736, s_l*4096 + 3072);
    dma16(r_w2, voffW2, s_l*128 + 114688, s_l*4096 + 16384);
    dma16(r_w2, voffW2, s_l*128 + 118784, s_l*4096 + 17408);
    dma16(r_w2, voffW2, s_l*128 + 122880, s_l*4096 + 18432);
    dma16(r_w2, voffW2, s_l*128 + 126976, s_l*4096 + 19456);
    WAITVM(18); LBAR();
    dma16(r_w2, voffW2, s_l*128 + 245760, s_l*4096 + 81920);
    dma16(r_w2, voffW2, s_l*128 + 249856, s_l*4096 + 82944);
    dma16(r_w2, voffW2, s_l*128 + 253952, s_l*4096 + 83968);
    dma16(r_w2, voffW2, s_l*128 + 258048, s_l*4096 + 84992);
    WAITVM(12); LBAR();
    dma16(r_w2, voffW2, s_l*128 + 376832, s_l*4096 + 98304);
    dma16(r_w2, voffW2, s_l*128 + 380928, s_l*4096 + 99328);
    dma16(r_w2, voffW2, s_l*128 + 385024, s_l*4096 + 100352);
    dma16(r_w2, voffW2, s_l*128 + 389120, s_l*4096 + 101376);
    WAITVM(8); LBAR();
    dma16(r_wf, voffW2, s_l*128 + 0, s_l*4096 + 114688);
    dma16(r_wf, voffW2, s_l*128 + 4096, s_l*4096 + 115712);
    dma16(r_wf, voffW2, s_l*128 + 8192, s_l*4096 + 116736);
    WAITVM(3); LBAR();
    dma16(r_wf, voffW2, s_l*128 + 12288, s_l*4096 + 117760);
    dma16(r_wf, voffW2, s_l*128 + 16384, s_l*4096 + 131072);
    dma16(r_wf, voffW2, s_l*128 + 20480, s_l*4096 + 132096);
    dma16(r_wf, voffW2, s_l*128 + 24576, s_l*4096 + 133120);
    dma16(r_wf, voffW2, s_l*128 + 28672, s_l*4096 + 134144);
     LBAR();
    dma16(r_wf, voffW2, s_l*128 + 32768, s_l*4096 + 147456);
    dma16(r_wf, voffW2, s_l*128 + 36864, s_l*4096 + 148480);
    dma16(r_wf, voffW2, s_l*128 + 40960, s_l*4096 + 149504);
    dma16(r_wf, voffW2, s_l*128 + 45056, s_l*4096 + 150528);
    dma16(r_wf, voffW2, s_l*128 + 49152, s_l*4096 + 0);
    dma16(r_wf, voffW2, s_l*128 + 53248, s_l*4096 + 1024);
    dma16(r_wf, voffW2, s_l*128 + 57344, s_l*4096 + 2048);
    dma16(r_wf, voffW2, s_l*128 + 61440, s_l*4096 + 3072);
    WAITVM(12); LBAR();
    dma16(r_wf, voffW2, s_l*128 + 65536, s_l*4096 + 16384);
    dma16(r_wf, voffW2, s_l*128 + 69632, s_l*4096 + 17408);
    dma16(r_wf, voffW2, s_l*128 + 73728, s_l*4096 + 18432);
    dma16(r_wf, voffW2, s_l*128 + 77824, s_l*4096 + 19456);
    dma16(r_wf, voffW2, s_l*128 + 81920, s_l*4096 + 81920);
    dma16(r_wf, voffW2, s_l*128 + 86016, s_l*4096 + 82944);
    dma16(r_wf, voffW2, s_l*128 + 90112, s_l*4096 + 83968);
    dma16(r_wf, voffW2, s_l*128 + 94208, s_l*4096 + 84992);
    dma16(r_wf, voffW2, s_l*128 + 98304, s_l*4096 + 98304);
    dma16(r_wf, voffW2, s_l*128 + 102400, s_l*4096 + 99328);
    WAITVM(18); LBAR();
    GE_WRITE();
    dma16(r_wf, voffW2, s_l*128 + 106496, s_l*4096 + 100352);
    dma16(r_wf, voffW2, s_l*128 + 110592, s_l*4096 + 101376);
    WAITVM(12); LBAR();
    dma16(r_wf, voffW2, s_l*128 + 114688, s_l*4096 + 114688);
    dma16(r_wf, voffW2, s_l*128 + 118784, s_l*4096 + 115712);
    dma16(r_wf, voffW2, s_l*128 + 122880, s_l*4096 + 116736);
    dma16(r_wf, voffW2, s_l*128 + 126976, s_l*4096 + 117760);
    dma16(r_wo, voffWo, s_l*16384 + 0, s_l*4096 + 131072);
    dma16(r_wo, voffWo, s_l*16384 + 4096, s_l*4096 + 132096);
    WAITVM(10); LBAR();
    dma16(r_wo, voffWo, s_l*16384 + 8192, s_l*4096 + 133120);
    dma16(r_wo, voffWo, s_l*16384 + 12288, s_l*4096 + 134144);
    dma16(r_wo, voffWo, s_l*16384 + 65536, s_l*4096 + 0);
    dma16(r_wo, voffWo, s_l*16384 + 69632, s_l*4096 + 1024);
    WAITVM(6); LBAR();
    dma16(r_wo, voffWo, s_l*16384 + 73728, s_l*4096 + 2048);
    dma16(r_wo, voffWo, s_l*16384 + 77824, s_l*4096 + 3072);
    dma16(r_wo, voffWo, s_l*16384 + 128, s_l*4096 + 147456);
    dma16(r_wo, voffWo, s_l*16384 + 4224, s_l*4096 + 148480);
     LBAR();
    dma16(r_wo, voffWo, s_l*16384 + 8320, s_l*4096 + 149504);
    dma16(r_wo, voffWo, s_l*16384 + 12416, s_l*4096 + 150528);
    dma16(r_wo, voffWo, s_l*16384 + 65664, s_l*4096 + 16384);
    dma16(r_wo, voffWo, s_l*16384 + 69760, s_l*4096 + 17408);
     LBAR();
    dma16(r_wo, voffWo, s_l*16384 + 73856, s_l*4096 + 18432);
    dma16(r_wo, voffWo, s_l*16384 + 77952, s_l*4096 + 19456);
    WAITVM(8);     _Pragma("unroll") for(int i=0;i<4;i++) _Pragma("unroll") for(int c=0;c<2;c++) acc[i][c]=(f32x4){0.f,0.f,0.f,0.f};
    __builtin_amdgcn_s_setprio(2);
  } else {

        const int col=lane&15, q=lane>>4, a_=(lane&15)>>2, p_=lane&3, w=wid;
    if(w<2){ int un=w*32+(sig&31); if(un<48) touch(mkrsrc(w1+un*8192,8192), lane*128, 70656+w*256); }
    else if(w==2 && CODE_UNITS && sig>=1 && sig<=CODE_UNITS){ touch(mkrsrc(code0,code_sz), (sig-1)*8192+lane*128, TSCR2); }
    gu64* hs_ack=(gu64*)(ws+WS_ACK)+tile;
    unsigned hs_r=__hip_atomic_load((gu32*)hs_ack, RLX_AGENT); unsigned long long hs_a;
    const int arow=4*q+a_, ag=(arow>>1)&3;
    int ab[3][4];
    _Pragma("unroll") for(int i=0;i<4;i++){ int t=w*4096+arow*128+((i^ag)*32)+8*p_; ab[0][i]=t; ab[1][i]=t+65536; ab[2][i]=t+131072; OPQ(ab[0][i]); OPQ(ab[1][i]); OPQ(ab[2][i]); }
    #define UX(i) (((i)>>3)*1024+((i)&7)*64+((q^((((i)>>2)&1)<<1))*16))
    int bx=UX(col), xs1=UX(col+15);
    int xs0 = col ? (3*4096+UX(col-1)) : (65536+192+q*16);   const int xs0a = col ? 512 : 256, xs0b = col ? 15872 : 256;
    int bh = (HZERO && col) ? ZBASE : 65536+q*16;
    if(HZERO) *(LAS u32x2*)(lds+ZBASE+threadIdx.x*8)=(u32x2){0u,0u};
    OPQ(bx); OPQ(xs1); OPQ(bh);
    const int t32=lane&31, h32=lane>>5, bl32=(lane>>4)&1;
    int a2[3][2];
    _Pragma("unroll") for(int m=0;m<2;m++){ int t=w*4096+(4*h32+a_)*128+(((2*m+bl32)^(((a_>>1)&1)<<1))*32)+8*p_; a2[0][m]=t; a2[1][m]=t+65536; a2[2][m]=t+131072; OPQ(a2[0][m]); OPQ(a2[1][m]); OPQ(a2[2][m]); }
    f32x16 ac2[2];
    _Pragma("unroll") for(int i=0;i<4;i++) _Pragma("unroll") for(int c=0;c<5;c++) acc[i][c]=(f32x4){0.f,0.f,0.f,0.f};
    if(threadIdx.x==0) *(lds_u32_p)(lds+69632)=0u;
    u32x4 hp[8]; f32x4 ep[8]; float hv, efm=0.f;

    BAR();
        s16x4 Al0_0=TR(ab[1][0]+16384);
    s16x4 Ah0_0=TR(ab[1][0]+18432);
    s16x8 B0_0=RD(xs0);
    s16x8 B0_1=RD(xs1+12288);
    s16x8 B0_2=RD(bx+4096);
    s16x8 B0_3=RD(bx+6144);
    s16x8 B0_4=RD(bh+64);
    s16x4 Al0_1=TR(ab[1][1]+16384);
    s16x4 Ah0_1=TR(ab[1][1]+18432);
    s16x4 Al0_2=TR(ab[1][2]+16384);
    s16x4 Ah0_2=TR(ab[1][2]+18432);
    s16x4 Al0_3=TR(ab[1][3]+16384);
    s16x4 Ah0_3=TR(ab[1][3]+18432);
    BAR();
    hs_r+=1u; if(threadIdx.x==0) __hip_atomic_store((gu32*)(ws+WS_REQ)+tile, hs_r, RLX_AGENT); SB;
    MF16(acc[0][0],Al0_0,Ah0_0,B0_0); s16x4 Al1_0=TR(ab[1][0]+32768); SB;
    MF16(acc[0][1],Al0_0,Ah0_0,B0_1); s16x4 Ah1_0=TR(ab[1][0]+34816); SB;
    MF16(acc[0][2],Al0_0,Ah0_0,B0_2); s16x8 B1_2=RD(bx+12288); SB;
    MF16(acc[0][3],Al0_0,Ah0_0,B0_3); s16x8 B1_3=RD(bx+14336); SB;
    MF16(acc[0][4],Al0_0,Ah0_0,B0_4); s16x8 B1_4=RD(bh+192); SB;
    MF16(acc[1][0],Al0_1,Ah0_1,B0_0); s16x4 Al1_1=TR(ab[1][1]+32768); SB;
    MF16(acc[1][1],Al0_1,Ah0_1,B0_1); s16x4 Ah1_1=TR(ab[1][1]+34816); SB;
    MF16(acc[1][2],Al0_1,Ah0_1,B0_2); s16x4 Al1_2=TR(ab[1][2]+32768); SB;
    MF16(acc[1][3],Al0_1,Ah0_1,B0_3); s16x4 Ah1_2=TR(ab[1][2]+34816); SB;
    MF16(acc[1][4],Al0_1,Ah0_1,B0_4); s16x4 Al1_3=TR(ab[1][3]+32768); SB;
    MF16(acc[2][0],Al0_2,Ah0_2,B0_0); s16x4 Ah1_3=TR(ab[1][3]+34816); SB;
    MF16(acc[2][1],Al0_2,Ah0_2,B0_1);  SB;
    MF16(acc[2][2],Al0_2,Ah0_2,B0_2);  SB;
    MF16(acc[2][3],Al0_2,Ah0_2,B0_3);  SB;
    MF16(acc[2][4],Al0_2,Ah0_2,B0_4);  SB;
    MF16(acc[3][0],Al0_3,Ah0_3,B0_0);  SB;
    MF16(acc[3][1],Al0_3,Ah0_3,B0_1);  SB;
    MF16(acc[3][2],Al0_3,Ah0_3,B0_2);  SB;
    MF16(acc[3][3],Al0_3,Ah0_3,B0_3);  SB;
    MF16(acc[3][4],Al0_3,Ah0_3,B0_4);  SB;
    BAR();
    MF16(acc[0][0],Al1_0,Ah1_0,B0_2); s16x4 Al2_0=TR(ab[1][0]+49152); SB;
    MF16(acc[0][1],Al1_0,Ah1_0,B0_3); s16x4 Ah2_0=TR(ab[1][0]+51200); SB;
    MF16(acc[0][2],Al1_0,Ah1_0,B1_2); s16x8 B2_0=RD(bx+0); SB;
    MF16(acc[0][3],Al1_0,Ah1_0,B1_3); s16x8 B2_1=RD(bx+2048); SB;
    MF16(acc[0][4],Al1_0,Ah1_0,B1_4); s16x8 B2_2=RD(bx+8192); SB;
    MF16(acc[1][0],Al1_1,Ah1_1,B0_2); s16x8 B2_3=RD(bx+10240); SB;
    MF16(acc[1][1],Al1_1,Ah1_1,B0_3); s16x8 B2_4=RD(bh+128); SB;
    MF16(acc[1][2],Al1_1,Ah1_1,B1_2); s16x4 Al2_1=TR(ab[1][1]+49152); SB;
    MF16(acc[1][3],Al1_1,Ah1_1,B1_3); s16x4 Ah2_1=TR(ab[1][1]+51200); SB;
    MF16(acc[1][4],Al1_1,Ah1_1,B1_4); s16x4 Al2_2=TR(ab[1][2]+49152); SB;
    MF16(acc[2][0],Al1_2,Ah1_2,B0_2); s16x4 Ah2_2=TR(ab[1][2]+51200); SB;
    MF16(acc[2][1],Al1_2,Ah1_2,B0_3); s16x4 Al2_3=TR(ab[1][3]+49152); SB;
    MF16(acc[2][2],Al1_2,Ah1_2,B1_2); s16x4 Ah2_3=TR(ab[1][3]+51200); SB;
    MF16(acc[2][3],Al1_2,Ah1_2,B1_3);  SB;
    MF16(acc[2][4],Al1_2,Ah1_2,B1_4);  SB;
    MF16(acc[3][0],Al1_3,Ah1_3,B0_2);  SB;
    MF16(acc[3][1],Al1_3,Ah1_3,B0_3);  SB;
    MF16(acc[3][2],Al1_3,Ah1_3,B1_2);  SB;
    MF16(acc[3][3],Al1_3,Ah1_3,B1_3);  SB;
    MF16(acc[3][4],Al1_3,Ah1_3,B1_4);  SB;
    BAR();
    MF16(acc[0][0],Al2_0,Ah2_0,B2_0); s16x4 Al3_0=TR(ab[2][0]+0); SB;
    MF16(acc[0][1],Al2_0,Ah2_0,B2_1); s16x4 Ah3_0=TR(ab[2][0]+2048); SB;
    MF16(acc[0][2],Al2_0,Ah2_0,B2_2); xs0+=xs0a; s16x8 B3_0=RD(xs0); SB;
    MF16(acc[0][3],Al2_0,Ah2_0,B2_3); s16x8 B3_1=RD(xs1+12800); SB;
    MF16(acc[0][4],Al2_0,Ah2_0,B2_4); s16x8 B3_2=RD(bx+4608); SB;
    MF16(acc[1][0],Al2_1,Ah2_1,B2_0); s16x8 B3_3=RD(bx+6656); SB;
    MF16(acc[1][1],Al2_1,Ah2_1,B2_1); s16x8 B3_4=RD(bh+320); SB;
    MF16(acc[1][2],Al2_1,Ah2_1,B2_2); s16x4 Al3_1=TR(ab[2][1]+0); SB;
    MF16(acc[1][3],Al2_1,Ah2_1,B2_3); s16x4 Ah3_1=TR(ab[2][1]+2048); SB;
    MF16(acc[1][4],Al2_1,Ah2_1,B2_4); s16x4 Al3_2=TR(ab[2][2]+0); SB;
    MF16(acc[2][0],Al2_2,Ah2_2,B2_0); s16x4 Ah3_2=TR(ab[2][2]+2048); SB;
    MF16(acc[2][1],Al2_2,Ah2_2,B2_1); s16x4 Al3_3=TR(ab[2][3]+0); SB;
    MF16(acc[2][2],Al2_2,Ah2_2,B2_2); s16x4 Ah3_3=TR(ab[2][3]+2048); SB;
    MF16(acc[2][3],Al2_2,Ah2_2,B2_3);  SB;
    MF16(acc[2][4],Al2_2,Ah2_2,B2_4);  SB;
    MF16(acc[3][0],Al2_3,Ah2_3,B2_0);  SB;
    MF16(acc[3][1],Al2_3,Ah2_3,B2_1);  SB;
    MF16(acc[3][2],Al2_3,Ah2_3,B2_2);  SB;
    MF16(acc[3][3],Al2_3,Ah2_3,B2_3);  SB;
    MF16(acc[3][4],Al2_3,Ah2_3,B2_4);  SB;
    BAR();
    MF16(acc[0][0],Al3_0,Ah3_0,B3_0); s16x4 Al4_0=TR(ab[2][0]+16384); SB;
    MF16(acc[0][1],Al3_0,Ah3_0,B3_1); s16x4 Ah4_0=TR(ab[2][0]+18432); SB;
    MF16(acc[0][2],Al3_0,Ah3_0,B3_2); s16x8 B4_2=RD(bx+12800); SB;
    MF16(acc[0][3],Al3_0,Ah3_0,B3_3); s16x8 B4_3=RD(bx+14848); SB;
    MF16(acc[0][4],Al3_0,Ah3_0,B3_4); s16x8 B4_4=RD(bh+448); SB;
    MF16(acc[1][0],Al3_1,Ah3_1,B3_0); s16x4 Al4_1=TR(ab[2][1]+16384); SB;
    MF16(acc[1][1],Al3_1,Ah3_1,B3_1); s16x4 Ah4_1=TR(ab[2][1]+18432); SB;
    MF16(acc[1][2],Al3_1,Ah3_1,B3_2); s16x4 Al4_2=TR(ab[2][2]+16384); SB;
    MF16(acc[1][3],Al3_1,Ah3_1,B3_3); s16x4 Ah4_2=TR(ab[2][2]+18432); SB;
    MF16(acc[1][4],Al3_1,Ah3_1,B3_4); s16x4 Al4_3=TR(ab[2][3]+16384); SB;
    MF16(acc[2][0],Al3_2,Ah3_2,B3_0); s16x4 Ah4_3=TR(ab[2][3]+18432); SB;
    MF16(acc[2][1],Al3_2,Ah3_2,B3_1);  SB;
    MF16(acc[2][2],Al3_2,Ah3_2,B3_2);  SB;
    MF16(acc[2][3],Al3_2,Ah3_2,B3_3);  SB;
    MF16(acc[2][4],Al3_2,Ah3_2,B3_4);  SB;
    MF16(acc[3][0],Al3_3,Ah3_3,B3_0);  SB;
    MF16(acc[3][1],Al3_3,Ah3_3,B3_1);  SB;
    MF16(acc[3][2],Al3_3,Ah3_3,B3_2);  SB;
    MF16(acc[3][3],Al3_3,Ah3_3,B3_3);  SB;
    MF16(acc[3][4],Al3_3,Ah3_3,B3_4);  SB;
    BAR();
    MF16(acc[0][0],Al4_0,Ah4_0,B3_2); s16x4 Al5_0=TR(ab[1][0]+16384); SB;
    MF16(acc[0][1],Al4_0,Ah4_0,B3_3); s16x4 Ah5_0=TR(ab[1][0]+18432); SB;
    MF16(acc[0][2],Al4_0,Ah4_0,B4_2); s16x8 B5_0=RD(bx+512); SB;
    MF16(acc[0][3],Al4_0,Ah4_0,B4_3); s16x8 B5_1=RD(bx+2560); SB;
    MF16(acc[0][4],Al4_0,Ah4_0,B4_4); s16x8 B5_2=RD(bx+8704); SB;
    MF16(acc[1][0],Al4_1,Ah4_1,B3_2); s16x8 B5_3=RD(bx+10752); SB;
    MF16(acc[1][1],Al4_1,Ah4_1,B3_3); s16x8 B5_4=RD(bh+384); SB;
    MF16(acc[1][2],Al4_1,Ah4_1,B4_2); s16x4 Al5_1=TR(ab[1][1]+16384); SB;
    MF16(acc[1][3],Al4_1,Ah4_1,B4_3); s16x4 Ah5_1=TR(ab[1][1]+18432); SB;
    MF16(acc[1][4],Al4_1,Ah4_1,B4_4); s16x4 Al5_2=TR(ab[1][2]+16384); SB;
    MF16(acc[2][0],Al4_2,Ah4_2,B3_2); s16x4 Ah5_2=TR(ab[1][2]+18432); SB;
    MF16(acc[2][1],Al4_2,Ah4_2,B3_3); s16x4 Al5_3=TR(ab[1][3]+16384); SB;
    MF16(acc[2][2],Al4_2,Ah4_2,B4_2); s16x4 Ah5_3=TR(ab[1][3]+18432); SB;
    MF16(acc[2][3],Al4_2,Ah4_2,B4_3);  SB;
    MF16(acc[2][4],Al4_2,Ah4_2,B4_4);  SB;
    MF16(acc[3][0],Al4_3,Ah4_3,B3_2);  SB;
    MF16(acc[3][1],Al4_3,Ah4_3,B3_3);  SB;
    MF16(acc[3][2],Al4_3,Ah4_3,B4_2);  SB;
    MF16(acc[3][3],Al4_3,Ah4_3,B4_3);  SB;
    MF16(acc[3][4],Al4_3,Ah4_3,B4_4);  SB;
    BAR();
    MF16(acc[0][0],Al5_0,Ah5_0,B5_0); s16x4 Al6_0=TR(ab[1][0]+32768); SB;
    MF16(acc[0][1],Al5_0,Ah5_0,B5_1); s16x4 Ah6_0=TR(ab[1][0]+34816); SB;
    MF16(acc[0][2],Al5_0,Ah5_0,B5_2); xs0+=xs0b; s16x8 B6_0=RD(xs0); SB;
    MF16(acc[0][3],Al5_0,Ah5_0,B5_3); s16x8 B6_1=RD(xs1+28672); SB;
    MF16(acc[0][4],Al5_0,Ah5_0,B5_4); s16x8 B6_2=RD(bx+20480); SB;
    MF16(acc[1][0],Al5_1,Ah5_1,B5_0); s16x8 B6_3=RD(bx+22528); SB;
    MF16(acc[1][1],Al5_1,Ah5_1,B5_1); s16x8 B6_4=RD(bh+576); SB;
    MF16(acc[1][2],Al5_1,Ah5_1,B5_2); s16x4 Al6_1=TR(ab[1][1]+32768); SB;
    MF16(acc[1][3],Al5_1,Ah5_1,B5_3); s16x4 Ah6_1=TR(ab[1][1]+34816); SB;
    MF16(acc[1][4],Al5_1,Ah5_1,B5_4); s16x4 Al6_2=TR(ab[1][2]+32768); SB;
    MF16(acc[2][0],Al5_2,Ah5_2,B5_0); s16x4 Ah6_2=TR(ab[1][2]+34816); SB;
    MF16(acc[2][1],Al5_2,Ah5_2,B5_1); s16x4 Al6_3=TR(ab[1][3]+32768); SB;
    MF16(acc[2][2],Al5_2,Ah5_2,B5_2); s16x4 Ah6_3=TR(ab[1][3]+34816); SB;
    MF16(acc[2][3],Al5_2,Ah5_2,B5_3);  SB;
    MF16(acc[2][4],Al5_2,Ah5_2,B5_4);  SB;
    MF16(acc[3][0],Al5_3,Ah5_3,B5_0);  SB;
    MF16(acc[3][1],Al5_3,Ah5_3,B5_1);  SB;
    MF16(acc[3][2],Al5_3,Ah5_3,B5_2);  SB;
    MF16(acc[3][3],Al5_3,Ah5_3,B5_3);  SB;
    MF16(acc[3][4],Al5_3,Ah5_3,B5_4);  SB;
    BAR();
    { int un=48+w*32+(sig&31); if(w<3 && un<128){ const char* tp = un<96 ? w2+(un-48)*8192 : un<112 ? wf+(un-96)*8192 : wo+(un-112)*8192; touch(mkrsrc(tp,8192), lane*128, 70656+w*256); } } SB;
    MF16(acc[0][0],Al6_0,Ah6_0,B6_0); s16x4 Al7_0=TR(ab[1][0]+49152); SB;
    MF16(acc[0][1],Al6_0,Ah6_0,B6_1); s16x4 Ah7_0=TR(ab[1][0]+51200); SB;
    MF16(acc[0][2],Al6_0,Ah6_0,B6_2); s16x8 B7_2=RD(bx+28672); SB;
    MF16(acc[0][3],Al6_0,Ah6_0,B6_3); s16x8 B7_3=RD(bx+30720); SB;
    MF16(acc[0][4],Al6_0,Ah6_0,B6_4); s16x8 B7_4=RD(bh+704); SB;
    MF16(acc[1][0],Al6_1,Ah6_1,B6_0); s16x4 Al7_1=TR(ab[1][1]+49152); SB;
    MF16(acc[1][1],Al6_1,Ah6_1,B6_1); s16x4 Ah7_1=TR(ab[1][1]+51200); SB;
    MF16(acc[1][2],Al6_1,Ah6_1,B6_2); s16x4 Al7_2=TR(ab[1][2]+49152); SB;
    MF16(acc[1][3],Al6_1,Ah6_1,B6_3); s16x4 Ah7_2=TR(ab[1][2]+51200); SB;
    MF16(acc[1][4],Al6_1,Ah6_1,B6_4); s16x4 Al7_3=TR(ab[1][3]+49152); SB;
    MF16(acc[2][0],Al6_2,Ah6_2,B6_0); s16x4 Ah7_3=TR(ab[1][3]+51200); SB;
    MF16(acc[2][1],Al6_2,Ah6_2,B6_1);  SB;
    MF16(acc[2][2],Al6_2,Ah6_2,B6_2);  SB;
    MF16(acc[2][3],Al6_2,Ah6_2,B6_3);  SB;
    MF16(acc[2][4],Al6_2,Ah6_2,B6_4);  SB;
    MF16(acc[3][0],Al6_3,Ah6_3,B6_0);  SB;
    MF16(acc[3][1],Al6_3,Ah6_3,B6_1);  SB;
    MF16(acc[3][2],Al6_3,Ah6_3,B6_2);  SB;
    MF16(acc[3][3],Al6_3,Ah6_3,B6_3);  SB;
    MF16(acc[3][4],Al6_3,Ah6_3,B6_4);  SB;
    BAR();
    MF16(acc[0][0],Al7_0,Ah7_0,B6_2); s16x4 Al8_0=TR(ab[2][0]+0); SB;
    MF16(acc[0][1],Al7_0,Ah7_0,B6_3); s16x4 Ah8_0=TR(ab[2][0]+2048); SB;
    MF16(acc[0][2],Al7_0,Ah7_0,B7_2); s16x8 B8_0=RD(bx+16384); SB;
    MF16(acc[0][3],Al7_0,Ah7_0,B7_3); s16x8 B8_1=RD(bx+18432); SB;
    MF16(acc[0][4],Al7_0,Ah7_0,B7_4); s16x8 B8_2=RD(bx+24576); SB;
    MF16(acc[1][0],Al7_1,Ah7_1,B6_2); s16x8 B8_3=RD(bx+26624); SB;
    MF16(acc[1][1],Al7_1,Ah7_1,B6_3); s16x8 B8_4=RD(bh+640); SB;
    MF16(acc[1][2],Al7_1,Ah7_1,B7_2); s16x4 Al8_1=TR(ab[2][1]+0); SB;
    MF16(acc[1][3],Al7_1,Ah7_1,B7_3); s16x4 Ah8_1=TR(ab[2][1]+2048); SB;
    MF16(acc[1][4],Al7_1,Ah7_1,B7_4); s16x4 Al8_2=TR(ab[2][2]+0); SB;
    MF16(acc[2][0],Al7_2,Ah7_2,B6_2); s16x4 Ah8_2=TR(ab[2][2]+2048); SB;
    MF16(acc[2][1],Al7_2,Ah7_2,B6_3); s16x4 Al8_3=TR(ab[2][3]+0); SB;
    MF16(acc[2][2],Al7_2,Ah7_2,B7_2); s16x4 Ah8_3=TR(ab[2][3]+2048); SB;
    MF16(acc[2][3],Al7_2,Ah7_2,B7_3);  SB;
    MF16(acc[2][4],Al7_2,Ah7_2,B7_4);  SB;
    MF16(acc[3][0],Al7_3,Ah7_3,B6_2);  SB;
    MF16(acc[3][1],Al7_3,Ah7_3,B6_3);  SB;
    MF16(acc[3][2],Al7_3,Ah7_3,B7_2);  SB;
    MF16(acc[3][3],Al7_3,Ah7_3,B7_3);  SB;
    MF16(acc[3][4],Al7_3,Ah7_3,B7_4);  SB;
    BAR();
    MF16(acc[0][0],Al8_0,Ah8_0,B8_0); s16x4 Al9_0=TR(ab[2][0]+16384); SB;
    MF16(acc[0][1],Al8_0,Ah8_0,B8_1); s16x4 Ah9_0=TR(ab[2][0]+18432); SB;
    MF16(acc[0][2],Al8_0,Ah8_0,B8_2); xs0+=xs0a; s16x8 B9_0=RD(xs0); SB;
    MF16(acc[0][3],Al8_0,Ah8_0,B8_3); s16x8 B9_1=RD(xs1+29184); SB;
    MF16(acc[0][4],Al8_0,Ah8_0,B8_4); s16x8 B9_2=RD(bx+20992); SB;
    MF16(acc[1][0],Al8_1,Ah8_1,B8_0); s16x8 B9_3=RD(bx+23040); SB;
    MF16(acc[1][1],Al8_1,Ah8_1,B8_1); s16x8 B9_4=RD(bh+832); SB;
    MF16(acc[1][2],Al8_1,Ah8_1,B8_2); s16x4 Al9_1=TR(ab[2][1]+16384); SB;
    MF16(acc[1][3],Al8_1,Ah8_1,B8_3); s16x4 Ah9_1=TR(ab[2][1]+18432); SB;
    MF16(acc[1][4],Al8_1,Ah8_1,B8_4); s16x4 Al9_2=TR(ab[2][2]+16384); SB;
    MF16(acc[2][0],Al8_2,Ah8_2,B8_0); s16x4 Ah9_2=TR(ab[2][2]+18432); SB;
    MF16(acc[2][1],Al8_2,Ah8_2,B8_1); s16x4 Al9_3=TR(ab[2][3]+16384); SB;
    MF16(acc[2][2],Al8_2,Ah8_2,B8_2); s16x4 Ah9_3=TR(ab[2][3]+18432); SB;
    MF16(acc[2][3],Al8_2,Ah8_2,B8_3);  SB;
    MF16(acc[2][4],Al8_2,Ah8_2,B8_4);  SB;
    MF16(acc[3][0],Al8_3,Ah8_3,B8_0);  SB;
    MF16(acc[3][1],Al8_3,Ah8_3,B8_1);  SB;
    MF16(acc[3][2],Al8_3,Ah8_3,B8_2);  SB;
    MF16(acc[3][3],Al8_3,Ah8_3,B8_3);  SB;
    MF16(acc[3][4],Al8_3,Ah8_3,B8_4);  SB;
    BAR();
    MF16(acc[0][0],Al9_0,Ah9_0,B9_0); s16x4 Al10_0=TR(ab[0][0]+0); SB;
    MF16(acc[0][1],Al9_0,Ah9_0,B9_1); s16x4 Ah10_0=TR(ab[0][0]+2048); SB;
    MF16(acc[0][2],Al9_0,Ah9_0,B9_2); s16x8 B10_2=RD(bx+29184); SB;
    MF16(acc[0][3],Al9_0,Ah9_0,B9_3); s16x8 B10_3=RD(bx+31232); SB;
    MF16(acc[0][4],Al9_0,Ah9_0,B9_4); s16x8 B10_4=RD(bh+960); SB;
    MF16(acc[1][0],Al9_1,Ah9_1,B9_0); s16x4 Al10_1=TR(ab[0][1]+0); SB;
    MF16(acc[1][1],Al9_1,Ah9_1,B9_1); s16x4 Ah10_1=TR(ab[0][1]+2048); SB;
    MF16(acc[1][2],Al9_1,Ah9_1,B9_2); s16x4 Al10_2=TR(ab[0][2]+0); SB;
    MF16(acc[1][3],Al9_1,Ah9_1,B9_3); s16x4 Ah10_2=TR(ab[0][2]+2048); SB;
    MF16(acc[1][4],Al9_1,Ah9_1,B9_4); s16x4 Al10_3=TR(ab[0][3]+0); SB;
    MF16(acc[2][0],Al9_2,Ah9_2,B9_0); s16x4 Ah10_3=TR(ab[0][3]+2048); SB;
    MF16(acc[2][1],Al9_2,Ah9_2,B9_1);  SB;
    MF16(acc[2][2],Al9_2,Ah9_2,B9_2);  SB;
    MF16(acc[2][3],Al9_2,Ah9_2,B9_3);  SB;
    MF16(acc[2][4],Al9_2,Ah9_2,B9_4);  SB;
    MF16(acc[3][0],Al9_3,Ah9_3,B9_0);  SB;
    MF16(acc[3][1],Al9_3,Ah9_3,B9_1);  SB;
    MF16(acc[3][2],Al9_3,Ah9_3,B9_2);  SB;
    MF16(acc[3][3],Al9_3,Ah9_3,B9_3);  SB;
    MF16(acc[3][4],Al9_3,Ah9_3,B9_4);  SB;
    BAR();
    MF16(acc[0][0],Al10_0,Ah10_0,B9_2); s16x4 Al11_0=TR(ab[1][0]+16384); SB;
    MF16(acc[0][1],Al10_0,Ah10_0,B9_3); s16x4 Ah11_0=TR(ab[1][0]+18432); SB;
    MF16(acc[0][2],Al10_0,Ah10_0,B10_2); s16x8 B11_0=RD(bx+16896); SB;
    MF16(acc[0][3],Al10_0,Ah10_0,B10_3); s16x8 B11_1=RD(bx+18944); SB;
    MF16(acc[0][4],Al10_0,Ah10_0,B10_4); s16x8 B11_2=RD(bx+25088); SB;
    MF16(acc[1][0],Al10_1,Ah10_1,B9_2); s16x8 B11_3=RD(bx+27136); SB;
    MF16(acc[1][1],Al10_1,Ah10_1,B9_3); s16x8 B11_4=RD(bh+896); SB;
    MF16(acc[1][2],Al10_1,Ah10_1,B10_2); s16x4 Al11_1=TR(ab[1][1]+16384); SB;
    MF16(acc[1][3],Al10_1,Ah10_1,B10_3); s16x4 Ah11_1=TR(ab[1][1]+18432); SB;
    MF16(acc[1][4],Al10_1,Ah10_1,B10_4); s16x4 Al11_2=TR(ab[1][2]+16384); SB;
    MF16(acc[2][0],Al10_2,Ah10_2,B9_2); s16x4 Ah11_2=TR(ab[1][2]+18432); SB;
    MF16(acc[2][1],Al10_2,Ah10_2,B9_3); s16x4 Al11_3=TR(ab[1][3]+16384); SB;
    MF16(acc[2][2],Al10_2,Ah10_2,B10_2); s16x4 Ah11_3=TR(ab[1][3]+18432); SB;
    MF16(acc[2][3],Al10_2,Ah10_2,B10_3);  SB;
    MF16(acc[2][4],Al10_2,Ah10_2,B10_4);  SB;
    MF16(acc[3][0],Al10_3,Ah10_3,B9_2);  SB;
    MF16(acc[3][1],Al10_3,Ah10_3,B9_3);  SB;
    MF16(acc[3][2],Al10_3,Ah10_3,B10_2);  SB;
    MF16(acc[3][3],Al10_3,Ah10_3,B10_3);  SB;
    MF16(acc[3][4],Al10_3,Ah10_3,B10_4);  SB;
    BAR();
    MF16(acc[0][0],Al11_0,Ah11_0,B11_0); s16x4 Al12_0=TR(ab[1][0]+32768); SB;
    MF16(acc[0][1],Al11_0,Ah11_0,B11_1); s16x4 Ah12_0=TR(ab[1][0]+34816); SB;
    MF16(acc[0][2],Al11_0,Ah11_0,B11_2); xs0+=xs0b; s16x8 B12_0=RD(xs0); SB;
    MF16(acc[0][3],Al11_0,Ah11_0,B11_3); s16x8 B12_1=RD(xs1+45056); SB;
    MF16(acc[0][4],Al11_0,Ah11_0,B11_4); s16x8 B12_2=RD(bx+36864); SB;
    MF16(acc[1][0],Al11_1,Ah11_1,B11_0); s16x8 B12_3=RD(bx+38912); SB;
    MF16(acc[1][1],Al11_1,Ah11_1,B11_1); s16x8 B12_4=RD(bh+1088); SB;
    MF16(acc[1][2],Al11_1,Ah11_1,B11_2); s16x4 Al12_1=TR(ab[1][1]+32768); SB;
    MF16(acc[1][3],Al11_1,Ah11_1,B11_3); s16x4 Ah12_1=TR(ab[1][1]+34816); SB;
    MF16(acc[1][4],Al11_1,Ah11_1,B11_4); s16x4 Al12_2=TR(ab[1][2]+32768); SB;
    MF16(acc[2][0],Al11_2,Ah11_2,B11_0); s16x4 Ah12_2=TR(ab[1][2]+34816); SB;
    MF16(acc[2][1],Al11_2,Ah11_2,B11_1); s16x4 Al12_3=TR(ab[1][3]+32768); SB;
    MF16(acc[2][2],Al11_2,Ah11_2,B11_2); s16x4 Ah12_3=TR(ab[1][3]+34816); SB;
    MF16(acc[2][3],Al11_2,Ah11_2,B11_3);  SB;
    MF16(acc[2][4],Al11_2,Ah11_2,B11_4);  SB;
    MF16(acc[3][0],Al11_3,Ah11_3,B11_0);  SB;
    MF16(acc[3][1],Al11_3,Ah11_3,B11_1);  SB;
    MF16(acc[3][2],Al11_3,Ah11_3,B11_2);  SB;
    MF16(acc[3][3],Al11_3,Ah11_3,B11_3);  SB;
    MF16(acc[3][4],Al11_3,Ah11_3,B11_4);  SB;
    BAR();
        MF16(acc[0][0],Al12_0,Ah12_0,B12_0); s16x4 Al13_0=TR(ab[1][0]+49152); SB;
    MF16(acc[0][1],Al12_0,Ah12_0,B12_1); s16x4 Ah13_0=TR(ab[1][0]+51200); SB;
    MF16(acc[0][2],Al12_0,Ah12_0,B12_2); s16x8 B13_2=RD(bx+45056); SB;
    MF16(acc[0][3],Al12_0,Ah12_0,B12_3); s16x8 B13_3=RD(bx+47104); SB;
    MF16(acc[0][4],Al12_0,Ah12_0,B12_4); s16x8 B13_4=RD(bh+1216); SB;
    MF16(acc[1][0],Al12_1,Ah12_1,B12_0); s16x4 Al13_1=TR(ab[1][1]+49152); SB;
    MF16(acc[1][1],Al12_1,Ah12_1,B12_1); s16x4 Ah13_1=TR(ab[1][1]+51200); SB;
    MF16(acc[1][2],Al12_1,Ah12_1,B12_2); s16x4 Al13_2=TR(ab[1][2]+49152); SB;
    MF16(acc[1][3],Al12_1,Ah12_1,B12_3); s16x4 Ah13_2=TR(ab[1][2]+51200); SB;
    MF16(acc[1][4],Al12_1,Ah12_1,B12_4); s16x4 Al13_3=TR(ab[1][3]+49152); SB;
    MF16(acc[2][0],Al12_2,Ah12_2,B12_0); s16x4 Ah13_3=TR(ab[1][3]+51200); SB;
    MF16(acc[2][1],Al12_2,Ah12_2,B12_1);  SB;
    MF16(acc[2][2],Al12_2,Ah12_2,B12_2);  SB;
    MF16(acc[2][3],Al12_2,Ah12_2,B12_3);  SB;
    MF16(acc[2][4],Al12_2,Ah12_2,B12_4);  SB;
    MF16(acc[3][0],Al12_3,Ah12_3,B12_0);  SB;
    MF16(acc[3][1],Al12_3,Ah12_3,B12_1);  SB;
    MF16(acc[3][2],Al12_3,Ah12_3,B12_2);  SB;
    MF16(acc[3][3],Al12_3,Ah12_3,B12_3);  SB;
    MF16(acc[3][4],Al12_3,Ah12_3,B12_4);  SB;
    BAR();
    MF16(acc[0][0],Al13_0,Ah13_0,B12_2); s16x4 Al14_0=TR(ab[2][0]+0); SB;
    MF16(acc[0][1],Al13_0,Ah13_0,B12_3); s16x4 Ah14_0=TR(ab[2][0]+2048); SB;
    MF16(acc[0][2],Al13_0,Ah13_0,B13_2); s16x8 B14_0=RD(bx+32768); SB;
    MF16(acc[0][3],Al13_0,Ah13_0,B13_3); s16x8 B14_1=RD(bx+34816); SB;
    MF16(acc[0][4],Al13_0,Ah13_0,B13_4); s16x8 B14_2=RD(bx+40960); SB;
    MF16(acc[1][0],Al13_1,Ah13_1,B12_2); s16x8 B14_3=RD(bx+43008); SB;
    MF16(acc[1][1],Al13_1,Ah13_1,B12_3); s16x8 B14_4=RD(bh+1152); SB;
    MF16(acc[1][2],Al13_1,Ah13_1,B13_2); s16x4 Al14_1=TR(ab[2][1]+0); SB;
    MF16(acc[1][3],Al13_1,Ah13_1,B13_3); s16x4 Ah14_1=TR(ab[2][1]+2048); SB;
    MF16(acc[1][4],Al13_1,Ah13_1,B13_4); s16x4 Al14_2=TR(ab[2][2]+0); SB;
    MF16(acc[2][0],Al13_2,Ah13_2,B12_2); s16x4 Ah14_2=TR(ab[2][2]+2048); SB;
    MF16(acc[2][1],Al13_2,Ah13_2,B12_3); s16x4 Al14_3=TR(ab[2][3]+0); SB;
    MF16(acc[2][2],Al13_2,Ah13_2,B13_2); s16x4 Ah14_3=TR(ab[2][3]+2048); SB;
    MF16(acc[2][3],Al13_2,Ah13_2,B13_3);  SB;
    MF16(acc[2][4],Al13_2,Ah13_2,B13_4);  SB;
    MF16(acc[3][0],Al13_3,Ah13_3,B12_2);  SB;
    MF16(acc[3][1],Al13_3,Ah13_3,B12_3);  SB;
    MF16(acc[3][2],Al13_3,Ah13_3,B13_2);  SB;
    MF16(acc[3][3],Al13_3,Ah13_3,B13_3);  SB;
    MF16(acc[3][4],Al13_3,Ah13_3,B13_4);  SB;
    BAR();
    MF16(acc[0][0],Al14_0,Ah14_0,B14_0); s16x4 Al15_0=TR(ab[2][0]+16384); SB;
    MF16(acc[0][1],Al14_0,Ah14_0,B14_1); s16x4 Ah15_0=TR(ab[2][0]+18432); SB;
    MF16(acc[0][2],Al14_0,Ah14_0,B14_2); xs0+=xs0a; s16x8 B15_0=RD(xs0); SB;
    MF16(acc[0][3],Al14_0,Ah14_0,B14_3); s16x8 B15_1=RD(xs1+45568); SB;
    MF16(acc[0][4],Al14_0,Ah14_0,B14_4); s16x8 B15_2=RD(bx+37376); SB;
    MF16(acc[1][0],Al14_1,Ah14_1,B14_0); s16x8 B15_3=RD(bx+39424); SB;
    MF16(acc[1][1],Al14_1,Ah14_1,B14_1); s16x8 B15_4=RD(bh+1344); SB;
    MF16(acc[1][2],Al14_1,Ah14_1,B14_2); s16x4 Al15_1=TR(ab[2][1]+16384); SB;
    MF16(acc[1][3],Al14_1,Ah14_1,B14_3); s16x4 Ah15_1=TR(ab[2][1]+18432); SB;
    MF16(acc[1][4],Al14_1,Ah14_1,B14_4); s16x4 Al15_2=TR(ab[2][2]+16384); SB;
    MF16(acc[2][0],Al14_2,Ah14_2,B14_0); s16x4 Ah15_2=TR(ab[2][2]+18432); SB;
    MF16(acc[2][1],Al14_2,Ah14_2,B14_1); s16x4 Al15_3=TR(ab[2][3]+16384); SB;
    MF16(acc[2][2],Al14_2,Ah14_2,B14_2); s16x4 Ah15_3=TR(ab[2][3]+18432); SB;
    MF16(acc[2][3],Al14_2,Ah14_2,B14_3);  SB;
    MF16(acc[2][4],Al14_2,Ah14_2,B14_4);  SB;
    MF16(acc[3][0],Al14_3,Ah14_3,B14_0);  SB;
    MF16(acc[3][1],Al14_3,Ah14_3,B14_1);  SB;
    MF16(acc[3][2],Al14_3,Ah14_3,B14_2);  SB;
    MF16(acc[3][3],Al14_3,Ah14_3,B14_3);  SB;
    MF16(acc[3][4],Al14_3,Ah14_3,B14_4);  SB;
    BAR();
    MF16(acc[0][0],Al15_0,Ah15_0,B15_0); s16x4 Al16_0=TR(ab[0][0]+0); SB;
    MF16(acc[0][1],Al15_0,Ah15_0,B15_1); s16x4 Ah16_0=TR(ab[0][0]+2048); SB;
    MF16(acc[0][2],Al15_0,Ah15_0,B15_2); s16x8 B16_2=RD(bx+45568); SB;
    MF16(acc[0][3],Al15_0,Ah15_0,B15_3); s16x8 B16_3=RD(bx+47616); SB;
    MF16(acc[0][4],Al15_0,Ah15_0,B15_4); s16x8 B16_4=RD(bh+1472); SB;
    MF16(acc[1][0],Al15_1,Ah15_1,B15_0); s16x4 Al16_1=TR(ab[0][1]+0); SB;
    MF16(acc[1][1],Al15_1,Ah15_1,B15_1); s16x4 Ah16_1=TR(ab[0][1]+2048); SB;
    MF16(acc[1][2],Al15_1,Ah15_1,B15_2); s16x4 Al16_2=TR(ab[0][2]+0); SB;
    MF16(acc[1][3],Al15_1,Ah15_1,B15_3); s16x4 Ah16_2=TR(ab[0][2]+2048); SB;
    MF16(acc[1][4],Al15_1,Ah15_1,B15_4); s16x4 Al16_3=TR(ab[0][3]+0); SB;
    MF16(acc[2][0],Al15_2,Ah15_2,B15_0); s16x4 Ah16_3=TR(ab[0][3]+2048); SB;
    MF16(acc[2][1],Al15_2,Ah15_2,B15_1);  SB;
    MF16(acc[2][2],Al15_2,Ah15_2,B15_2);  SB;
    MF16(acc[2][3],Al15_2,Ah15_2,B15_3);  SB;
    MF16(acc[2][4],Al15_2,Ah15_2,B15_4);  SB;
    MF16(acc[3][0],Al15_3,Ah15_3,B15_0);  SB;
    MF16(acc[3][1],Al15_3,Ah15_3,B15_1);  SB;
    MF16(acc[3][2],Al15_3,Ah15_3,B15_2);  SB;
    MF16(acc[3][3],Al15_3,Ah15_3,B15_3);  SB;
    MF16(acc[3][4],Al15_3,Ah15_3,B15_4);  SB;
    BAR();
    MF16(acc[0][0],Al16_0,Ah16_0,B15_2); s16x4 Al17_0=TR(ab[0][0]+16384); SB;
    MF16(acc[0][1],Al16_0,Ah16_0,B15_3); s16x4 Ah17_0=TR(ab[0][0]+18432); SB;
    MF16(acc[0][2],Al16_0,Ah16_0,B16_2); s16x8 B17_0=RD(bx+33280); SB;
    MF16(acc[0][3],Al16_0,Ah16_0,B16_3); s16x8 B17_1=RD(bx+35328); SB;
    MF16(acc[0][4],Al16_0,Ah16_0,B16_4); s16x8 B17_2=RD(bx+41472); SB;
    MF16(acc[1][0],Al16_1,Ah16_1,B15_2); s16x8 B17_3=RD(bx+43520); SB;
    MF16(acc[1][1],Al16_1,Ah16_1,B15_3); s16x8 B17_4=RD(bh+1408); SB;
    MF16(acc[1][2],Al16_1,Ah16_1,B16_2); s16x4 Al17_1=TR(ab[0][1]+16384); SB;
    MF16(acc[1][3],Al16_1,Ah16_1,B16_3); s16x4 Ah17_1=TR(ab[0][1]+18432); SB;
    MF16(acc[1][4],Al16_1,Ah16_1,B16_4); s16x4 Al17_2=TR(ab[0][2]+16384); SB;
    MF16(acc[2][0],Al16_2,Ah16_2,B15_2); s16x4 Ah17_2=TR(ab[0][2]+18432); SB;
    MF16(acc[2][1],Al16_2,Ah16_2,B15_3); s16x4 Al17_3=TR(ab[0][3]+16384); SB;
    MF16(acc[2][2],Al16_2,Ah16_2,B16_2); s16x4 Ah17_3=TR(ab[0][3]+18432); SB;
    MF16(acc[2][3],Al16_2,Ah16_2,B16_3);  SB;
    MF16(acc[2][4],Al16_2,Ah16_2,B16_4);  SB;
    MF16(acc[3][0],Al16_3,Ah16_3,B15_2);  SB;
    MF16(acc[3][1],Al16_3,Ah16_3,B15_3);  SB;
    MF16(acc[3][2],Al16_3,Ah16_3,B16_2);  SB;
    MF16(acc[3][3],Al16_3,Ah16_3,B16_3);  SB;
    MF16(acc[3][4],Al16_3,Ah16_3,B16_4);  SB;
    BAR();
    MF16(acc[0][0],Al17_0,Ah17_0,B17_0); s16x4 Al18_0=TR(ab[1][0]+16384); SB;
    MF16(acc[0][1],Al17_0,Ah17_0,B17_1); s16x4 Ah18_0=TR(ab[1][0]+18432); SB;
    MF16(acc[0][2],Al17_0,Ah17_0,B17_2); xs0+=xs0b; s16x8 B18_0=RD(xs0); SB;
    MF16(acc[0][3],Al17_0,Ah17_0,B17_3); s16x8 B18_1=RD(xs1+61440); SB;
    MF16(acc[0][4],Al17_0,Ah17_0,B17_4); s16x8 B18_2=RD(bx+53248); SB;
    MF16(acc[1][0],Al17_1,Ah17_1,B17_0); s16x8 B18_3=RD(bx+55296); SB;
    MF16(acc[1][1],Al17_1,Ah17_1,B17_1); s16x8 B18_4=RD(bh+1600); SB;
    MF16(acc[1][2],Al17_1,Ah17_1,B17_2); s16x4 Al18_1=TR(ab[1][1]+16384); SB;
    MF16(acc[1][3],Al17_1,Ah17_1,B17_3); s16x4 Ah18_1=TR(ab[1][1]+18432); SB;
    MF16(acc[1][4],Al17_1,Ah17_1,B17_4); s16x4 Al18_2=TR(ab[1][2]+16384); SB;
    MF16(acc[2][0],Al17_2,Ah17_2,B17_0); s16x4 Ah18_2=TR(ab[1][2]+18432); SB;
    MF16(acc[2][1],Al17_2,Ah17_2,B17_1); s16x4 Al18_3=TR(ab[1][3]+16384); SB;
    MF16(acc[2][2],Al17_2,Ah17_2,B17_2); s16x4 Ah18_3=TR(ab[1][3]+18432); SB;
    MF16(acc[2][3],Al17_2,Ah17_2,B17_3);  SB;
    MF16(acc[2][4],Al17_2,Ah17_2,B17_4);  SB;
    MF16(acc[3][0],Al17_3,Ah17_3,B17_0);  SB;
    MF16(acc[3][1],Al17_3,Ah17_3,B17_1);  SB;
    MF16(acc[3][2],Al17_3,Ah17_3,B17_2);  SB;
    MF16(acc[3][3],Al17_3,Ah17_3,B17_3);  SB;
    MF16(acc[3][4],Al17_3,Ah17_3,B17_4);  SB;
    BAR();
    MF16(acc[0][0],Al18_0,Ah18_0,B18_0); s16x4 Al19_0=TR(ab[1][0]+32768); SB;
    MF16(acc[0][1],Al18_0,Ah18_0,B18_1); s16x4 Ah19_0=TR(ab[1][0]+34816); SB;
    MF16(acc[0][2],Al18_0,Ah18_0,B18_2); s16x8 B19_2=RD(bx+61440); SB;
    MF16(acc[0][3],Al18_0,Ah18_0,B18_3); s16x8 B19_3=RD(bx+63488); SB;
    MF16(acc[0][4],Al18_0,Ah18_0,B18_4); s16x8 B19_4=RD(bh+1728); SB;
    MF16(acc[1][0],Al18_1,Ah18_1,B18_0); s16x4 Al19_1=TR(ab[1][1]+32768); SB;
    MF16(acc[1][1],Al18_1,Ah18_1,B18_1); s16x4 Ah19_1=TR(ab[1][1]+34816); SB;
    MF16(acc[1][2],Al18_1,Ah18_1,B18_2); s16x4 Al19_2=TR(ab[1][2]+32768); SB;
    MF16(acc[1][3],Al18_1,Ah18_1,B18_3); s16x4 Ah19_2=TR(ab[1][2]+34816); SB;
    MF16(acc[1][4],Al18_1,Ah18_1,B18_4); s16x4 Al19_3=TR(ab[1][3]+32768); SB;
    MF16(acc[2][0],Al18_2,Ah18_2,B18_0); s16x4 Ah19_3=TR(ab[1][3]+34816); SB;
    MF16(acc[2][1],Al18_2,Ah18_2,B18_1);  SB;
    MF16(acc[2][2],Al18_2,Ah18_2,B18_2);  SB;
    MF16(acc[2][3],Al18_2,Ah18_2,B18_3);  SB;
    MF16(acc[2][4],Al18_2,Ah18_2,B18_4);  SB;
    MF16(acc[3][0],Al18_3,Ah18_3,B18_0);  SB;
    MF16(acc[3][1],Al18_3,Ah18_3,B18_1);  SB;
    MF16(acc[3][2],Al18_3,Ah18_3,B18_2);  SB;
    MF16(acc[3][3],Al18_3,Ah18_3,B18_3);  SB;
    MF16(acc[3][4],Al18_3,Ah18_3,B18_4);  SB;
    BAR();
    MF16(acc[0][0],Al19_0,Ah19_0,B18_2); s16x4 Al20_0=TR(ab[1][0]+49152); SB;
    MF16(acc[0][1],Al19_0,Ah19_0,B18_3); s16x4 Ah20_0=TR(ab[1][0]+51200); SB;
    MF16(acc[0][2],Al19_0,Ah19_0,B19_2); s16x8 B20_0=RD(bx+49152); SB;
    MF16(acc[0][3],Al19_0,Ah19_0,B19_3); s16x8 B20_1=RD(bx+51200); SB;
    MF16(acc[0][4],Al19_0,Ah19_0,B19_4); s16x8 B20_2=RD(bx+57344); SB;
    MF16(acc[1][0],Al19_1,Ah19_1,B18_2); s16x8 B20_3=RD(bx+59392); SB;
    MF16(acc[1][1],Al19_1,Ah19_1,B18_3); s16x8 B20_4=RD(bh+1664); SB;
    MF16(acc[1][2],Al19_1,Ah19_1,B19_2); s16x4 Al20_1=TR(ab[1][1]+49152); SB;
    MF16(acc[1][3],Al19_1,Ah19_1,B19_3); s16x4 Ah20_1=TR(ab[1][1]+51200); SB;
    MF16(acc[1][4],Al19_1,Ah19_1,B19_4); s16x4 Al20_2=TR(ab[1][2]+49152); SB;
    MF16(acc[2][0],Al19_2,Ah19_2,B18_2); s16x4 Ah20_2=TR(ab[1][2]+51200); SB;
    MF16(acc[2][1],Al19_2,Ah19_2,B18_3); s16x4 Al20_3=TR(ab[1][3]+49152); SB;
    MF16(acc[2][2],Al19_2,Ah19_2,B19_2); s16x4 Ah20_3=TR(ab[1][3]+51200); SB;
    MF16(acc[2][3],Al19_2,Ah19_2,B19_3);  SB;
    MF16(acc[2][4],Al19_2,Ah19_2,B19_4);  SB;
    MF16(acc[3][0],Al19_3,Ah19_3,B18_2);  SB;
    MF16(acc[3][1],Al19_3,Ah19_3,B18_3);  SB;
    MF16(acc[3][2],Al19_3,Ah19_3,B19_2);  SB;
    MF16(acc[3][3],Al19_3,Ah19_3,B19_3);  SB;
    MF16(acc[3][4],Al19_3,Ah19_3,B19_4);  SB;
    BAR();
    MF16(acc[0][0],Al20_0,Ah20_0,B20_0); s16x4 Al21_0=TR(ab[2][0]+0); SB;
    MF16(acc[0][1],Al20_0,Ah20_0,B20_1); s16x4 Ah21_0=TR(ab[2][0]+2048); SB;
    MF16(acc[0][2],Al20_0,Ah20_0,B20_2); xs0+=xs0a; s16x8 B21_0=RD(xs0); SB;
    MF16(acc[0][3],Al20_0,Ah20_0,B20_3); s16x8 B21_1=RD(xs1+61952); SB;
    MF16(acc[0][4],Al20_0,Ah20_0,B20_4); s16x8 B21_2=RD(bx+53760); SB;
    MF16(acc[1][0],Al20_1,Ah20_1,B20_0); s16x8 B21_3=RD(bx+55808); SB;
    MF16(acc[1][1],Al20_1,Ah20_1,B20_1); s16x8 B21_4=RD(bh+1856); SB;
    MF16(acc[1][2],Al20_1,Ah20_1,B20_2); s16x4 Al21_1=TR(ab[2][1]+0); SB;
    MF16(acc[1][3],Al20_1,Ah20_1,B20_3); s16x4 Ah21_1=TR(ab[2][1]+2048); SB;
    MF16(acc[1][4],Al20_1,Ah20_1,B20_4); s16x4 Al21_2=TR(ab[2][2]+0); SB;
    MF16(acc[2][0],Al20_2,Ah20_2,B20_0); s16x4 Ah21_2=TR(ab[2][2]+2048); SB;
    MF16(acc[2][1],Al20_2,Ah20_2,B20_1); s16x4 Al21_3=TR(ab[2][3]+0); SB;
    MF16(acc[2][2],Al20_2,Ah20_2,B20_2); s16x4 Ah21_3=TR(ab[2][3]+2048); SB;
    MF16(acc[2][3],Al20_2,Ah20_2,B20_3);  SB;
    MF16(acc[2][4],Al20_2,Ah20_2,B20_4);  SB;
    MF16(acc[3][0],Al20_3,Ah20_3,B20_0);  SB;
    MF16(acc[3][1],Al20_3,Ah20_3,B20_1);  SB;
    MF16(acc[3][2],Al20_3,Ah20_3,B20_2);  SB;
    MF16(acc[3][3],Al20_3,Ah20_3,B20_3);  SB;
    MF16(acc[3][4],Al20_3,Ah20_3,B20_4);  SB;
    BAR();
    MF16(acc[0][0],Al21_0,Ah21_0,B21_0); s16x4 Al22_0=TR(ab[2][0]+16384); SB;
    MF16(acc[0][1],Al21_0,Ah21_0,B21_1); s16x4 Ah22_0=TR(ab[2][0]+18432); SB;
    MF16(acc[0][2],Al21_0,Ah21_0,B21_2); s16x8 B22_2=RD(bx+61952); SB;
    MF16(acc[0][3],Al21_0,Ah21_0,B21_3); s16x8 B22_3=RD(bx+64000); SB;
    MF16(acc[0][4],Al21_0,Ah21_0,B21_4); s16x8 B22_4=RD(bh+1984); SB;
    MF16(acc[1][0],Al21_1,Ah21_1,B21_0); s16x4 Al22_1=TR(ab[2][1]+16384); SB;
    MF16(acc[1][1],Al21_1,Ah21_1,B21_1); s16x4 Ah22_1=TR(ab[2][1]+18432); SB;
    MF16(acc[1][2],Al21_1,Ah21_1,B21_2); s16x4 Al22_2=TR(ab[2][2]+16384); SB;
    MF16(acc[1][3],Al21_1,Ah21_1,B21_3); s16x4 Ah22_2=TR(ab[2][2]+18432); SB;
    MF16(acc[1][4],Al21_1,Ah21_1,B21_4); s16x4 Al22_3=TR(ab[2][3]+16384); SB;
    MF16(acc[2][0],Al21_2,Ah21_2,B21_0); s16x4 Ah22_3=TR(ab[2][3]+18432); SB;
    MF16(acc[2][1],Al21_2,Ah21_2,B21_1);  SB;
    MF16(acc[2][2],Al21_2,Ah21_2,B21_2);  SB;
    MF16(acc[2][3],Al21_2,Ah21_2,B21_3);  SB;
    MF16(acc[2][4],Al21_2,Ah21_2,B21_4);  SB;
    MF16(acc[3][0],Al21_3,Ah21_3,B21_0);  SB;
    MF16(acc[3][1],Al21_3,Ah21_3,B21_1);  SB;
    MF16(acc[3][2],Al21_3,Ah21_3,B21_2);  SB;
    MF16(acc[3][3],Al21_3,Ah21_3,B21_3);  SB;
    MF16(acc[3][4],Al21_3,Ah21_3,B21_4);  SB;
    BAR();
    MF16(acc[0][0],Al22_0,Ah22_0,B21_2); s16x4 Al23_0=TR(ab[0][0]+0); SB;
    MF16(acc[0][1],Al22_0,Ah22_0,B21_3); s16x4 Ah23_0=TR(ab[0][0]+2048); SB;
    MF16(acc[0][2],Al22_0,Ah22_0,B22_2); s16x8 B23_0=RD(bx+49664); SB;
    MF16(acc[0][3],Al22_0,Ah22_0,B22_3); s16x8 B23_1=RD(bx+51712); SB;
    MF16(acc[0][4],Al22_0,Ah22_0,B22_4); s16x8 B23_2=RD(bx+57856); SB;
    MF16(acc[1][0],Al22_1,Ah22_1,B21_2); s16x8 B23_3=RD(bx+59904); SB;
    MF16(acc[1][1],Al22_1,Ah22_1,B21_3); s16x8 B23_4=RD(bh+1920); SB;
    MF16(acc[1][2],Al22_1,Ah22_1,B22_2); s16x4 Al23_1=TR(ab[0][1]+0); SB;
    MF16(acc[1][3],Al22_1,Ah22_1,B22_3); s16x4 Ah23_1=TR(ab[0][1]+2048); SB;
    MF16(acc[1][4],Al22_1,Ah22_1,B22_4); s16x4 Al23_2=TR(ab[0][2]+0); SB;
    MF16(acc[2][0],Al22_2,Ah22_2,B21_2); s16x4 Ah23_2=TR(ab[0][2]+2048); SB;
    MF16(acc[2][1],Al22_2,Ah22_2,B21_3); s16x4 Al23_3=TR(ab[0][3]+0); SB;
    MF16(acc[2][2],Al22_2,Ah22_2,B22_2); s16x4 Ah23_3=TR(ab[0][3]+2048); SB;
    MF16(acc[2][3],Al22_2,Ah22_2,B22_3);  SB;
    MF16(acc[2][4],Al22_2,Ah22_2,B22_4);  SB;
    MF16(acc[3][0],Al22_3,Ah22_3,B21_2);  SB;
    MF16(acc[3][1],Al22_3,Ah22_3,B21_3);  SB;
    MF16(acc[3][2],Al22_3,Ah22_3,B22_2);  SB;
    MF16(acc[3][3],Al22_3,Ah22_3,B22_3);  SB;
    MF16(acc[3][4],Al22_3,Ah22_3,B22_4);  SB;
    BAR();
    MF16(acc[0][0],Al23_0,Ah23_0,B23_0); s16x4 Al24_0=TR(a2[0][0]+16384); SB;
    MF16(acc[0][1],Al23_0,Ah23_0,B23_1); s16x4 Ah24_0=TR(a2[0][0]+18432); SB;
    MF16(acc[0][2],Al23_0,Ah23_0,B23_2); s16x4 Al24_1=TR(a2[0][1]+16384); SB;
    MF16(acc[0][3],Al23_0,Ah23_0,B23_3); s16x4 Ah24_1=TR(a2[0][1]+18432); SB;
    MF16(acc[0][4],Al23_0,Ah23_0,B23_4); s16x4 Al24_2=TR(a2[0][0]+17408); SB;
    MF16(acc[1][0],Al23_1,Ah23_1,B23_0); s16x4 Ah24_2=TR(a2[0][0]+19456); SB;
    MF16(acc[1][1],Al23_1,Ah23_1,B23_1); s16x4 Al24_3=TR(a2[0][1]+17408); SB;
    MF16(acc[1][2],Al23_1,Ah23_1,B23_2); s16x4 Ah24_3=TR(a2[0][1]+19456); SB;
    MF16(acc[1][3],Al23_1,Ah23_1,B23_3); s16x4 Al25_0=TR(a2[1][0]+16384); SB;
    MF16(acc[1][4],Al23_1,Ah23_1,B23_4); s16x4 Ah25_0=TR(a2[1][0]+18432); SB;
    MF16(acc[2][0],Al23_2,Ah23_2,B23_0); s16x4 Al25_1=TR(a2[1][1]+16384); SB;
    MF16(acc[2][1],Al23_2,Ah23_2,B23_1); s16x4 Ah25_1=TR(a2[1][1]+18432); SB;
    MF16(acc[2][2],Al23_2,Ah23_2,B23_2); s16x4 Al25_2=TR(a2[1][0]+17408); SB;
    MF16(acc[2][3],Al23_2,Ah23_2,B23_3); s16x4 Ah25_2=TR(a2[1][0]+19456); SB;
    MF16(acc[2][4],Al23_2,Ah23_2,B23_4); s16x4 Al25_3=TR(a2[1][1]+17408); SB;
    MF16(acc[3][0],Al23_3,Ah23_3,B23_0); s16x4 Ah25_3=TR(a2[1][1]+19456); SB;
    MF16(acc[3][1],Al23_3,Ah23_3,B23_1);  SB;
    MF16(acc[3][2],Al23_3,Ah23_3,B23_2);  SB;
    MF16(acc[3][3],Al23_3,Ah23_3,B23_3);  SB;
    MF16(acc[3][4],Al23_3,Ah23_3,B23_4);  SB;
    BAR();
        const GeluC gc=gelu_consts();
    s16x4 Al26_0=TR(a2[1][0]+32768); SB; f32x2 gt1_0=(f32x2){acc[0][0][0],acc[0][0][1]}*(f32x2){acc[0][0][0],acc[0][0][1]}; f32x2 gt1_1=(f32x2){acc[0][0][2],acc[0][0][3]}*(f32x2){acc[0][0][2],acc[0][0][3]}; f32x2 gt1_2=(f32x2){acc[1][0][0],acc[1][0][1]}*(f32x2){acc[1][0][0],acc[1][0][1]}; f32x2 gt1_3=(f32x2){acc[1][0][2],acc[1][0][3]}*(f32x2){acc[1][0][2],acc[1][0][3]}; SB;
    s16x4 Ah26_0=TR(a2[1][0]+34816); SB; gt1_0=__builtin_elementwise_fma(gt1_0,gc.c2,gc.c1); gt1_1=__builtin_elementwise_fma(gt1_1,gc.c2,gc.c1); gt1_2=__builtin_elementwise_fma(gt1_2,gc.c2,gc.c1); gt1_3=__builtin_elementwise_fma(gt1_3,gc.c2,gc.c1); SB;
     SB; gt1_0=gt1_0*(f32x2){acc[0][0][0],acc[0][0][1]}; gt1_1=gt1_1*(f32x2){acc[0][0][2],acc[0][0][3]}; gt1_2=gt1_2*(f32x2){acc[1][0][0],acc[1][0][1]}; gt1_3=gt1_3*(f32x2){acc[1][0][2],acc[1][0][3]}; SB;
    s16x4 Al26_1=TR(a2[1][1]+32768); SB; gt1_0[0]=__builtin_amdgcn_exp2f(gt1_0[0]); gt1_0[1]=__builtin_amdgcn_exp2f(gt1_0[1]); SB;
    s16x4 Ah26_1=TR(a2[1][1]+34816); SB; gt1_1[0]=__builtin_amdgcn_exp2f(gt1_1[0]); gt1_1[1]=__builtin_amdgcn_exp2f(gt1_1[1]); SB;
     SB; gt1_2[0]=__builtin_amdgcn_exp2f(gt1_2[0]); gt1_2[1]=__builtin_amdgcn_exp2f(gt1_2[1]); SB;
    s16x4 Al26_2=TR(a2[1][0]+33792); SB; gt1_3[0]=__builtin_amdgcn_exp2f(gt1_3[0]); gt1_3[1]=__builtin_amdgcn_exp2f(gt1_3[1]); SB;
     SB; gt1_0=gt1_0+gc.one; gt1_1=gt1_1+gc.one; gt1_2=gt1_2+gc.one; gt1_3=gt1_3+gc.one; SB;
    s16x4 Ah26_2=TR(a2[1][0]+35840); SB; gt1_0[0]=__builtin_amdgcn_rcpf(gt1_0[0]); gt1_0[1]=__builtin_amdgcn_rcpf(gt1_0[1]); SB;
    s16x4 Al26_3=TR(a2[1][1]+33792); SB; gt1_1[0]=__builtin_amdgcn_rcpf(gt1_1[0]); gt1_1[1]=__builtin_amdgcn_rcpf(gt1_1[1]); SB;
     SB; gt1_2[0]=__builtin_amdgcn_rcpf(gt1_2[0]); gt1_2[1]=__builtin_amdgcn_rcpf(gt1_2[1]); SB;
    s16x4 Ah26_3=TR(a2[1][1]+35840); SB; gt1_3[0]=__builtin_amdgcn_rcpf(gt1_3[0]); gt1_3[1]=__builtin_amdgcn_rcpf(gt1_3[1]); SB;
     SB; gt1_0=gt1_0*(f32x2){acc[0][0][0],acc[0][0][1]}; gt1_1=gt1_1*(f32x2){acc[0][0][2],acc[0][0][3]}; gt1_2=gt1_2*(f32x2){acc[1][0][0],acc[1][0][1]}; gt1_3=gt1_3*(f32x2){acc[1][0][2],acc[1][0][3]}; SB;
     SB; hp[0][0]=pk2(gt1_0[0],gt1_0[1]); hp[0][1]=pk2(gt1_1[0],gt1_1[1]); hp[0][2]=pk2(gt1_2[0],gt1_2[1]); hp[0][3]=pk2(gt1_3[0],gt1_3[1]); OPQ(hp[0]); SB;
    s16x4 Al27_0=TR(a2[1][0]+49152); SB; f32x2 gt2_0=(f32x2){acc[2][0][0],acc[2][0][1]}*(f32x2){acc[2][0][0],acc[2][0][1]}; f32x2 gt2_1=(f32x2){acc[2][0][2],acc[2][0][3]}*(f32x2){acc[2][0][2],acc[2][0][3]}; f32x2 gt2_2=(f32x2){acc[3][0][0],acc[3][0][1]}*(f32x2){acc[3][0][0],acc[3][0][1]}; f32x2 gt2_3=(f32x2){acc[3][0][2],acc[3][0][3]}*(f32x2){acc[3][0][2],acc[3][0][3]}; SB;
    s16x4 Ah27_0=TR(a2[1][0]+51200); SB; gt2_0=__builtin_elementwise_fma(gt2_0,gc.c2,gc.c1); gt2_1=__builtin_elementwise_fma(gt2_1,gc.c2,gc.c1); gt2_2=__builtin_elementwise_fma(gt2_2,gc.c2,gc.c1); gt2_3=__builtin_elementwise_fma(gt2_3,gc.c2,gc.c1); SB;
     SB; gt2_0=gt2_0*(f32x2){acc[2][0][0],acc[2][0][1]}; gt2_1=gt2_1*(f32x2){acc[2][0][2],acc[2][0][3]}; gt2_2=gt2_2*(f32x2){acc[3][0][0],acc[3][0][1]}; gt2_3=gt2_3*(f32x2){acc[3][0][2],acc[3][0][3]}; SB;
    s16x4 Al27_1=TR(a2[1][1]+49152); SB; gt2_0[0]=__builtin_amdgcn_exp2f(gt2_0[0]); gt2_0[1]=__builtin_amdgcn_exp2f(gt2_0[1]); SB;
    s16x4 Ah27_1=TR(a2[1][1]+51200); SB; gt2_1[0]=__builtin_amdgcn_exp2f(gt2_1[0]); gt2_1[1]=__builtin_amdgcn_exp2f(gt2_1[1]); SB;
     SB; gt2_2[0]=__builtin_amdgcn_exp2f(gt2_2[0]); gt2_2[1]=__builtin_amdgcn_exp2f(gt2_2[1]); SB;
    s16x4 Al27_2=TR(a2[1][0]+50176); SB; gt2_3[0]=__builtin_amdgcn_exp2f(gt2_3[0]); gt2_3[1]=__builtin_amdgcn_exp2f(gt2_3[1]); SB;
     SB; gt2_0=gt2_0+gc.one; gt2_1=gt2_1+gc.one; gt2_2=gt2_2+gc.one; gt2_3=gt2_3+gc.one; SB;
    s16x4 Ah27_2=TR(a2[1][0]+52224); SB; gt2_0[0]=__builtin_amdgcn_rcpf(gt2_0[0]); gt2_0[1]=__builtin_amdgcn_rcpf(gt2_0[1]); SB;
    s16x4 Al27_3=TR(a2[1][1]+50176); SB; gt2_1[0]=__builtin_amdgcn_rcpf(gt2_1[0]); gt2_1[1]=__builtin_amdgcn_rcpf(gt2_1[1]); SB;
     SB; gt2_2[0]=__builtin_amdgcn_rcpf(gt2_2[0]); gt2_2[1]=__builtin_amdgcn_rcpf(gt2_2[1]); SB;
    s16x4 Ah27_3=TR(a2[1][1]+52224); SB; gt2_3[0]=__builtin_amdgcn_rcpf(gt2_3[0]); gt2_3[1]=__builtin_amdgcn_rcpf(gt2_3[1]); SB;
     SB; gt2_0=gt2_0*(f32x2){acc[2][0][0],acc[2][0][1]}; gt2_1=gt2_1*(f32x2){acc[2][0][2],acc[2][0][3]}; gt2_2=gt2_2*(f32x2){acc[3][0][0],acc[3][0][1]}; gt2_3=gt2_3*(f32x2){acc[3][0][2],acc[3][0][3]}; SB;
     SB; hp[1][0]=pk2(gt2_0[0],gt2_0[1]); hp[1][1]=pk2(gt2_1[0],gt2_1[1]); hp[1][2]=pk2(gt2_2[0],gt2_2[1]); hp[1][3]=pk2(gt2_3[0],gt2_3[1]); OPQ(hp[1]); SB;
    BAR();
        s16x4 Al28_0=TR(a2[2][0]+0); s16x4 Ah28_0=TR(a2[2][0]+2048); SB; f32x2 gt3_0=(f32x2){acc[0][1][0],acc[0][1][1]}*(f32x2){acc[0][1][0],acc[0][1][1]}; f32x2 gt3_1=(f32x2){acc[0][1][2],acc[0][1][3]}*(f32x2){acc[0][1][2],acc[0][1][3]}; f32x2 gt3_2=(f32x2){acc[1][1][0],acc[1][1][1]}*(f32x2){acc[1][1][0],acc[1][1][1]}; f32x2 gt3_3=(f32x2){acc[1][1][2],acc[1][1][3]}*(f32x2){acc[1][1][2],acc[1][1][3]}; SB;
    s16x4 Al28_1=TR(a2[2][1]+0); SB; gt3_0=__builtin_elementwise_fma(gt3_0,gc.c2,gc.c1); gt3_1=__builtin_elementwise_fma(gt3_1,gc.c2,gc.c1); gt3_2=__builtin_elementwise_fma(gt3_2,gc.c2,gc.c1); gt3_3=__builtin_elementwise_fma(gt3_3,gc.c2,gc.c1); SB;
    s16x4 Ah28_1=TR(a2[2][1]+2048); s16x4 Al28_2=TR(a2[2][0]+1024); SB; gt3_0=gt3_0*(f32x2){acc[0][1][0],acc[0][1][1]}; gt3_1=gt3_1*(f32x2){acc[0][1][2],acc[0][1][3]}; gt3_2=gt3_2*(f32x2){acc[1][1][0],acc[1][1][1]}; gt3_3=gt3_3*(f32x2){acc[1][1][2],acc[1][1][3]}; SB;
    s16x4 Ah28_2=TR(a2[2][0]+3072); SB; gt3_0[0]=__builtin_amdgcn_exp2f(gt3_0[0]); gt3_0[1]=__builtin_amdgcn_exp2f(gt3_0[1]); SB;
    s16x4 Al28_3=TR(a2[2][1]+1024); s16x4 Ah28_3=TR(a2[2][1]+3072); SB; gt3_1[0]=__builtin_amdgcn_exp2f(gt3_1[0]); gt3_1[1]=__builtin_amdgcn_exp2f(gt3_1[1]); SB;
    f32x4 hs0_=hbc<1>(acc[0][4],acc[0][4]); OPQ(hs0_); SB; gt3_2[0]=__builtin_amdgcn_exp2f(gt3_2[0]); gt3_2[1]=__builtin_amdgcn_exp2f(gt3_2[1]); SB;
    hs0_=hbc<2>(hs0_,acc[1][4]); OPQ(hs0_); hs0_=hbc<4>(hs0_,acc[2][4]); OPQ(hs0_); SB; gt3_3[0]=__builtin_amdgcn_exp2f(gt3_3[0]); gt3_3[1]=__builtin_amdgcn_exp2f(gt3_3[1]); SB;
    hs0_=hbc<8>(hs0_,acc[3][4]); OPQ(hs0_); SB; gt3_0=gt3_0+gc.one; gt3_1=gt3_1+gc.one; gt3_2=gt3_2+gc.one; gt3_3=gt3_3+gc.one; SB;
    float hu0_=(col&1)?hs0_[1]:hs0_[0], hu1_=(col&1)?hs0_[3]:hs0_[2]; hv=(col&2)?hu1_:hu0_; float hw_=hv*hv; OPQ(hv); OPQ(hw_); hw_=hw_*gc.c2[0]+gc.c1[0]; OPQ(hw_); SB; gt3_0[0]=__builtin_amdgcn_rcpf(gt3_0[0]); gt3_0[1]=__builtin_amdgcn_rcpf(gt3_0[1]); SB;
    hw_=hw_*hv; OPQ(hw_); SB; gt3_1[0]=__builtin_amdgcn_rcpf(gt3_1[0]); gt3_1[1]=__builtin_amdgcn_rcpf(gt3_1[1]); SB;
    hw_=__builtin_amdgcn_exp2f(hw_); OPQ(hw_); hw_=hw_+1.0f; OPQ(hw_); SB; gt3_2[0]=__builtin_amdgcn_rcpf(gt3_2[0]); gt3_2[1]=__builtin_amdgcn_rcpf(gt3_2[1]); SB;
    hw_=__builtin_amdgcn_rcpf(hw_); OPQ(hw_); SB; gt3_3[0]=__builtin_amdgcn_rcpf(gt3_3[0]); gt3_3[1]=__builtin_amdgcn_rcpf(gt3_3[1]); SB;
    hv=hv*hw_; OPQ(hv); SB; gt3_0=gt3_0*(f32x2){acc[0][1][0],acc[0][1][1]}; gt3_1=gt3_1*(f32x2){acc[0][1][2],acc[0][1][3]}; gt3_2=gt3_2*(f32x2){acc[1][1][0],acc[1][1][1]}; gt3_3=gt3_3*(f32x2){acc[1][1][2],acc[1][1][3]}; SB;
     SB; hp[2][0]=pk2(gt3_0[0],gt3_0[1]); hp[2][1]=pk2(gt3_1[0],gt3_1[1]); hp[2][2]=pk2(gt3_2[0],gt3_2[1]); hp[2][3]=pk2(gt3_3[0],gt3_3[1]); OPQ(hp[2]); SB;
    s16x4 Al29_0=TR(a2[2][0]+16384); SB; f32x2 gt4_0=(f32x2){acc[2][1][0],acc[2][1][1]}*(f32x2){acc[2][1][0],acc[2][1][1]}; f32x2 gt4_1=(f32x2){acc[2][1][2],acc[2][1][3]}*(f32x2){acc[2][1][2],acc[2][1][3]}; f32x2 gt4_2=(f32x2){acc[3][1][0],acc[3][1][1]}*(f32x2){acc[3][1][0],acc[3][1][1]}; f32x2 gt4_3=(f32x2){acc[3][1][2],acc[3][1][3]}*(f32x2){acc[3][1][2],acc[3][1][3]}; SB;
    s16x4 Ah29_0=TR(a2[2][0]+18432); SB; gt4_0=__builtin_elementwise_fma(gt4_0,gc.c2,gc.c1); gt4_1=__builtin_elementwise_fma(gt4_1,gc.c2,gc.c1); gt4_2=__builtin_elementwise_fma(gt4_2,gc.c2,gc.c1); gt4_3=__builtin_elementwise_fma(gt4_3,gc.c2,gc.c1); SB;
     SB; gt4_0=gt4_0*(f32x2){acc[2][1][0],acc[2][1][1]}; gt4_1=gt4_1*(f32x2){acc[2][1][2],acc[2][1][3]}; gt4_2=gt4_2*(f32x2){acc[3][1][0],acc[3][1][1]}; gt4_3=gt4_3*(f32x2){acc[3][1][2],acc[3][1][3]}; SB;
    s16x4 Al29_1=TR(a2[2][1]+16384); SB; gt4_0[0]=__builtin_amdgcn_exp2f(gt4_0[0]); gt4_0[1]=__builtin_amdgcn_exp2f(gt4_0[1]); SB;
    s16x4 Ah29_1=TR(a2[2][1]+18432); SB; gt4_1[0]=__builtin_amdgcn_exp2f(gt4_1[0]); gt4_1[1]=__builtin_amdgcn_exp2f(gt4_1[1]); SB;
     SB; gt4_2[0]=__builtin_amdgcn_exp2f(gt4_2[0]); gt4_2[1]=__builtin_amdgcn_exp2f(gt4_2[1]); SB;
    s16x4 Al29_2=TR(a2[2][0]+17408); SB; gt4_3[0]=__builtin_amdgcn_exp2f(gt4_3[0]); gt4_3[1]=__builtin_amdgcn_exp2f(gt4_3[1]); SB;
     SB; gt4_0=gt4_0+gc.one; gt4_1=gt4_1+gc.one; gt4_2=gt4_2+gc.one; gt4_3=gt4_3+gc.one; SB;
    s16x4 Ah29_2=TR(a2[2][0]+19456); SB; gt4_0[0]=__builtin_amdgcn_rcpf(gt4_0[0]); gt4_0[1]=__builtin_amdgcn_rcpf(gt4_0[1]); SB;
    s16x4 Al29_3=TR(a2[2][1]+17408); SB; gt4_1[0]=__builtin_amdgcn_rcpf(gt4_1[0]); gt4_1[1]=__builtin_amdgcn_rcpf(gt4_1[1]); SB;
     SB; gt4_2[0]=__builtin_amdgcn_rcpf(gt4_2[0]); gt4_2[1]=__builtin_amdgcn_rcpf(gt4_2[1]); SB;
    s16x4 Ah29_3=TR(a2[2][1]+19456); SB; gt4_3[0]=__builtin_amdgcn_rcpf(gt4_3[0]); gt4_3[1]=__builtin_amdgcn_rcpf(gt4_3[1]); SB;
     SB; gt4_0=gt4_0*(f32x2){acc[2][1][0],acc[2][1][1]}; gt4_1=gt4_1*(f32x2){acc[2][1][2],acc[2][1][3]}; gt4_2=gt4_2*(f32x2){acc[3][1][0],acc[3][1][1]}; gt4_3=gt4_3*(f32x2){acc[3][1][2],acc[3][1][3]}; SB;
     SB; hp[3][0]=pk2(gt4_0[0],gt4_0[1]); hp[3][1]=pk2(gt4_1[0],gt4_1[1]); hp[3][2]=pk2(gt4_2[0],gt4_2[1]); hp[3][3]=pk2(gt4_3[0],gt4_3[1]); OPQ(hp[3]); SB;
    BAR();
        s16x4 Al30_0=TR(a2[0][0]+0); SB; f32x2 gt5_0=(f32x2){acc[0][2][0],acc[0][2][1]}*(f32x2){acc[0][2][0],acc[0][2][1]}; f32x2 gt5_1=(f32x2){acc[0][2][2],acc[0][2][3]}*(f32x2){acc[0][2][2],acc[0][2][3]}; f32x2 gt5_2=(f32x2){acc[1][2][0],acc[1][2][1]}*(f32x2){acc[1][2][0],acc[1][2][1]}; f32x2 gt5_3=(f32x2){acc[1][2][2],acc[1][2][3]}*(f32x2){acc[1][2][2],acc[1][2][3]}; SB;
    s16x4 Ah30_0=TR(a2[0][0]+2048); SB; gt5_0=__builtin_elementwise_fma(gt5_0,gc.c2,gc.c1); gt5_1=__builtin_elementwise_fma(gt5_1,gc.c2,gc.c1); gt5_2=__builtin_elementwise_fma(gt5_2,gc.c2,gc.c1); gt5_3=__builtin_elementwise_fma(gt5_3,gc.c2,gc.c1); SB;
     SB; gt5_0=gt5_0*(f32x2){acc[0][2][0],acc[0][2][1]}; gt5_1=gt5_1*(f32x2){acc[0][2][2],acc[0][2][3]}; gt5_2=gt5_2*(f32x2){acc[1][2][0],acc[1][2][1]}; gt5_3=gt5_3*(f32x2){acc[1][2][2],acc[1][2][3]}; SB;
    s16x4 Al30_1=TR(a2[0][1]+0); SB; gt5_0[0]=__builtin_amdgcn_exp2f(gt5_0[0]); gt5_0[1]=__builtin_amdgcn_exp2f(gt5_0[1]); SB;
    s16x4 Ah30_1=TR(a2[0][1]+2048); SB; gt5_1[0]=__builtin_amdgcn_exp2f(gt5_1[0]); gt5_1[1]=__builtin_amdgcn_exp2f(gt5_1[1]); SB;
     SB; gt5_2[0]=__builtin_amdgcn_exp2f(gt5_2[0]); gt5_2[1]=__builtin_amdgcn_exp2f(gt5_2[1]); SB;
    s16x4 Al30_2=TR(a2[0][0]+1024); SB; gt5_3[0]=__builtin_amdgcn_exp2f(gt5_3[0]); gt5_3[1]=__builtin_amdgcn_exp2f(gt5_3[1]); SB;
     SB; gt5_0=gt5_0+gc.one; gt5_1=gt5_1+gc.one; gt5_2=gt5_2+gc.one; gt5_3=gt5_3+gc.one; SB;
    s16x4 Ah30_2=TR(a2[0][0]+3072); SB; gt5_0[0]=__builtin_amdgcn_rcpf(gt5_0[0]); gt5_0[1]=__builtin_amdgcn_rcpf(gt5_0[1]); SB;
    s16x4 Al30_3=TR(a2[0][1]+1024); SB; gt5_1[0]=__builtin_amdgcn_rcpf(gt5_1[0]); gt5_1[1]=__builtin_amdgcn_rcpf(gt5_1[1]); SB;
     SB; gt5_2[0]=__builtin_amdgcn_rcpf(gt5_2[0]); gt5_2[1]=__builtin_amdgcn_rcpf(gt5_2[1]); SB;
    s16x4 Ah30_3=TR(a2[0][1]+3072); SB; gt5_3[0]=__builtin_amdgcn_rcpf(gt5_3[0]); gt5_3[1]=__builtin_amdgcn_rcpf(gt5_3[1]); SB;
     SB; gt5_0=gt5_0*(f32x2){acc[0][2][0],acc[0][2][1]}; gt5_1=gt5_1*(f32x2){acc[0][2][2],acc[0][2][3]}; gt5_2=gt5_2*(f32x2){acc[1][2][0],acc[1][2][1]}; gt5_3=gt5_3*(f32x2){acc[1][2][2],acc[1][2][3]}; SB;
     SB; hp[4][0]=pk2(gt5_0[0],gt5_0[1]); hp[4][1]=pk2(gt5_1[0],gt5_1[1]); hp[4][2]=pk2(gt5_2[0],gt5_2[1]); hp[4][3]=pk2(gt5_3[0],gt5_3[1]); OPQ(hp[4]); SB;
    s16x4 Al31_0=TR(a2[0][0]+16384); SB; f32x2 gt6_0=(f32x2){acc[2][2][0],acc[2][2][1]}*(f32x2){acc[2][2][0],acc[2][2][1]}; f32x2 gt6_1=(f32x2){acc[2][2][2],acc[2][2][3]}*(f32x2){acc[2][2][2],acc[2][2][3]}; f32x2 gt6_2=(f32x2){acc[3][2][0],acc[3][2][1]}*(f32x2){acc[3][2][0],acc[3][2][1]}; f32x2 gt6_3=(f32x2){acc[3][2][2],acc[3][2][3]}*(f32x2){acc[3][2][2],acc[3][2][3]}; SB;
    s16x4 Ah31_0=TR(a2[0][0]+18432); SB; gt6_0=__builtin_elementwise_fma(gt6_0,gc.c2,gc.c1); gt6_1=__builtin_elementwise_fma(gt6_1,gc.c2,gc.c1); gt6_2=__builtin_elementwise_fma(gt6_2,gc.c2,gc.c1); gt6_3=__builtin_elementwise_fma(gt6_3,gc.c2,gc.c1); SB;
     SB; gt6_0=gt6_0*(f32x2){acc[2][2][0],acc[2][2][1]}; gt6_1=gt6_1*(f32x2){acc[2][2][2],acc[2][2][3]}; gt6_2=gt6_2*(f32x2){acc[3][2][0],acc[3][2][1]}; gt6_3=gt6_3*(f32x2){acc[3][2][2],acc[3][2][3]}; SB;
    s16x4 Al31_1=TR(a2[0][1]+16384); SB; gt6_0[0]=__builtin_amdgcn_exp2f(gt6_0[0]); gt6_0[1]=__builtin_amdgcn_exp2f(gt6_0[1]); SB;
    s16x4 Ah31_1=TR(a2[0][1]+18432); SB; gt6_1[0]=__builtin_amdgcn_exp2f(gt6_1[0]); gt6_1[1]=__builtin_amdgcn_exp2f(gt6_1[1]); SB;
     SB; gt6_2[0]=__builtin_amdgcn_exp2f(gt6_2[0]); gt6_2[1]=__builtin_amdgcn_exp2f(gt6_2[1]); SB;
    s16x4 Al31_2=TR(a2[0][0]+17408); SB; gt6_3[0]=__builtin_amdgcn_exp2f(gt6_3[0]); gt6_3[1]=__builtin_amdgcn_exp2f(gt6_3[1]); SB;
     SB; gt6_0=gt6_0+gc.one; gt6_1=gt6_1+gc.one; gt6_2=gt6_2+gc.one; gt6_3=gt6_3+gc.one; SB;
    s16x4 Ah31_2=TR(a2[0][0]+19456); SB; gt6_0[0]=__builtin_amdgcn_rcpf(gt6_0[0]); gt6_0[1]=__builtin_amdgcn_rcpf(gt6_0[1]); SB;
    s16x4 Al31_3=TR(a2[0][1]+17408); SB; gt6_1[0]=__builtin_amdgcn_rcpf(gt6_1[0]); gt6_1[1]=__builtin_amdgcn_rcpf(gt6_1[1]); SB;
     SB; gt6_2[0]=__builtin_amdgcn_rcpf(gt6_2[0]); gt6_2[1]=__builtin_amdgcn_rcpf(gt6_2[1]); SB;
    s16x4 Ah31_3=TR(a2[0][1]+19456); SB; gt6_3[0]=__builtin_amdgcn_rcpf(gt6_3[0]); gt6_3[1]=__builtin_amdgcn_rcpf(gt6_3[1]); SB;
     SB; gt6_0=gt6_0*(f32x2){acc[2][2][0],acc[2][2][1]}; gt6_1=gt6_1*(f32x2){acc[2][2][2],acc[2][2][3]}; gt6_2=gt6_2*(f32x2){acc[3][2][0],acc[3][2][1]}; gt6_3=gt6_3*(f32x2){acc[3][2][2],acc[3][2][3]}; SB;
     SB; hp[5][0]=pk2(gt6_0[0],gt6_0[1]); hp[5][1]=pk2(gt6_1[0],gt6_1[1]); hp[5][2]=pk2(gt6_2[0],gt6_2[1]); hp[5][3]=pk2(gt6_3[0],gt6_3[1]); OPQ(hp[5]); SB;
    BAR();
        hs_a=__hip_atomic_load(hs_ack, RLX_AGENT); SB;
    int hw=32768+w*8192+col*64+((q^((col>>2)&3))*16); OPQ(hw);
    int b2[2], hs0[2]; const int hs0s = t32 ? 4096 : 64;
    _Pragma("unroll") for(int s_=0;s_<2;s_++){ b2[s_]=t32*64+(((2*s_+h32)^((t32>>2)&3))*16); hs0[s_]= t32 ? (32768+2048+(t32-1)*64+(((2*s_+h32)^(((t32-1)>>2)&3))*16)) : (67584+(2*s_+h32)*16); OPQ(b2[s_]); OPQ(hs0[s_]); }
    s16x4 Al32_0=TR(a2[1][0]+16384); SB; f32x2 gt7_0=(f32x2){acc[0][3][0],acc[0][3][1]}*(f32x2){acc[0][3][0],acc[0][3][1]}; f32x2 gt7_1=(f32x2){acc[0][3][2],acc[0][3][3]}*(f32x2){acc[0][3][2],acc[0][3][3]}; f32x2 gt7_2=(f32x2){acc[1][3][0],acc[1][3][1]}*(f32x2){acc[1][3][0],acc[1][3][1]}; f32x2 gt7_3=(f32x2){acc[1][3][2],acc[1][3][3]}*(f32x2){acc[1][3][2],acc[1][3][3]}; SB;
    s16x4 Ah32_0=TR(a2[1][0]+18432); SB; gt7_0=__builtin_elementwise_fma(gt7_0,gc.c2,gc.c1); gt7_1=__builtin_elementwise_fma(gt7_1,gc.c2,gc.c1); gt7_2=__builtin_elementwise_fma(gt7_2,gc.c2,gc.c1); gt7_3=__builtin_elementwise_fma(gt7_3,gc.c2,gc.c1); SB;
    s16x4 Al32_1=TR(a2[1][1]+16384); SB; gt7_0=gt7_0*(f32x2){acc[0][3][0],acc[0][3][1]}; gt7_1=gt7_1*(f32x2){acc[0][3][2],acc[0][3][3]}; gt7_2=gt7_2*(f32x2){acc[1][3][0],acc[1][3][1]}; gt7_3=gt7_3*(f32x2){acc[1][3][2],acc[1][3][3]}; SB;
    s16x4 Ah32_1=TR(a2[1][1]+18432); SB; gt7_0[0]=__builtin_amdgcn_exp2f(gt7_0[0]); gt7_0[1]=__builtin_amdgcn_exp2f(gt7_0[1]); SB;
    s16x4 Al32_2=TR(a2[1][0]+17408); SB; gt7_1[0]=__builtin_amdgcn_exp2f(gt7_1[0]); gt7_1[1]=__builtin_amdgcn_exp2f(gt7_1[1]); SB;
    s16x4 Ah32_2=TR(a2[1][0]+19456); SB; gt7_2[0]=__builtin_amdgcn_exp2f(gt7_2[0]); gt7_2[1]=__builtin_amdgcn_exp2f(gt7_2[1]); SB;
     SB; gt7_3[0]=__builtin_amdgcn_exp2f(gt7_3[0]); gt7_3[1]=__builtin_amdgcn_exp2f(gt7_3[1]); SB;
    s16x4 Al32_3=TR(a2[1][1]+17408); SB; gt7_0=gt7_0+gc.one; gt7_1=gt7_1+gc.one; gt7_2=gt7_2+gc.one; gt7_3=gt7_3+gc.one; SB;
    s16x4 Ah32_3=TR(a2[1][1]+19456); SB; gt7_0[0]=__builtin_amdgcn_rcpf(gt7_0[0]); gt7_0[1]=__builtin_amdgcn_rcpf(gt7_0[1]); SB;
    *(lds_u32x4_p)(lds+hw+0)=hp[0]; SB; gt7_1[0]=__builtin_amdgcn_rcpf(gt7_1[0]); gt7_1[1]=__builtin_amdgcn_rcpf(gt7_1[1]); SB;
    *(lds_u32x4_p)(lds+hw+4096)=hp[1]; SB; gt7_2[0]=__builtin_amdgcn_rcpf(gt7_2[0]); gt7_2[1]=__builtin_amdgcn_rcpf(gt7_2[1]); SB;
    *(lds_u32x4_p)(lds+hw+1024)=hp[2]; SB; gt7_3[0]=__builtin_amdgcn_rcpf(gt7_3[0]); gt7_3[1]=__builtin_amdgcn_rcpf(gt7_3[1]); SB;
     SB; gt7_0=gt7_0*(f32x2){acc[0][3][0],acc[0][3][1]}; gt7_1=gt7_1*(f32x2){acc[0][3][2],acc[0][3][3]}; gt7_2=gt7_2*(f32x2){acc[1][3][0],acc[1][3][1]}; gt7_3=gt7_3*(f32x2){acc[1][3][2],acc[1][3][3]}; SB;
     SB; hp[6][0]=pk2(gt7_0[0],gt7_0[1]); hp[6][1]=pk2(gt7_1[0],gt7_1[1]); hp[6][2]=pk2(gt7_2[0],gt7_2[1]); hp[6][3]=pk2(gt7_3[0],gt7_3[1]); OPQ(hp[6]); SB;
    *(lds_u32x4_p)(lds+hw+5120)=hp[3]; SB; f32x2 gt8_0=(f32x2){acc[2][3][0],acc[2][3][1]}*(f32x2){acc[2][3][0],acc[2][3][1]}; f32x2 gt8_1=(f32x2){acc[2][3][2],acc[2][3][3]}*(f32x2){acc[2][3][2],acc[2][3][3]}; f32x2 gt8_2=(f32x2){acc[3][3][0],acc[3][3][1]}*(f32x2){acc[3][3][0],acc[3][3][1]}; f32x2 gt8_3=(f32x2){acc[3][3][2],acc[3][3][3]}*(f32x2){acc[3][3][2],acc[3][3][3]}; SB;
     SB; gt8_0=__builtin_elementwise_fma(gt8_0,gc.c2,gc.c1); gt8_1=__builtin_elementwise_fma(gt8_1,gc.c2,gc.c1); gt8_2=__builtin_elementwise_fma(gt8_2,gc.c2,gc.c1); gt8_3=__builtin_elementwise_fma(gt8_3,gc.c2,gc.c1); SB;
    *(lds_u32x4_p)(lds+hw+2048)=hp[4]; SB; gt8_0=gt8_0*(f32x2){acc[2][3][0],acc[2][3][1]}; gt8_1=gt8_1*(f32x2){acc[2][3][2],acc[2][3][3]}; gt8_2=gt8_2*(f32x2){acc[3][3][0],acc[3][3][1]}; gt8_3=gt8_3*(f32x2){acc[3][3][2],acc[3][3][3]}; SB;
     SB; gt8_0[0]=__builtin_amdgcn_exp2f(gt8_0[0]); gt8_0[1]=__builtin_amdgcn_exp2f(gt8_0[1]); SB;
     SB; gt8_1[0]=__builtin_amdgcn_exp2f(gt8_1[0]); gt8_1[1]=__builtin_amdgcn_exp2f(gt8_1[1]); SB;
    *(lds_u32x4_p)(lds+hw+6144)=hp[5]; SB; gt8_2[0]=__builtin_amdgcn_exp2f(gt8_2[0]); gt8_2[1]=__builtin_amdgcn_exp2f(gt8_2[1]); SB;
     SB; gt8_3[0]=__builtin_amdgcn_exp2f(gt8_3[0]); gt8_3[1]=__builtin_amdgcn_exp2f(gt8_3[1]); SB;
    *(lds_u16_p)(lds+67584+64*(2*w+(col>>3))+q*16+(col&7)*2)=__builtin_bit_cast(unsigned short,(__bf16)hv); SB; gt8_0=gt8_0+gc.one; gt8_1=gt8_1+gc.one; gt8_2=gt8_2+gc.one; gt8_3=gt8_3+gc.one; SB;
     SB; gt8_0[0]=__builtin_amdgcn_rcpf(gt8_0[0]); gt8_0[1]=__builtin_amdgcn_rcpf(gt8_0[1]); SB;
     SB; gt8_1[0]=__builtin_amdgcn_rcpf(gt8_1[0]); gt8_1[1]=__builtin_amdgcn_rcpf(gt8_1[1]); SB;
    *(lds_u32x4_p)(lds+hw+3072)=hp[6]; SB; gt8_2[0]=__builtin_amdgcn_rcpf(gt8_2[0]); gt8_2[1]=__builtin_amdgcn_rcpf(gt8_2[1]); SB;
     SB; gt8_3[0]=__builtin_amdgcn_rcpf(gt8_3[0]); gt8_3[1]=__builtin_amdgcn_rcpf(gt8_3[1]); SB;
     SB; gt8_0=gt8_0*(f32x2){acc[2][3][0],acc[2][3][1]}; gt8_1=gt8_1*(f32x2){acc[2][3][2],acc[2][3][3]}; gt8_2=gt8_2*(f32x2){acc[3][3][0],acc[3][3][1]}; gt8_3=gt8_3*(f32x2){acc[3][3][2],acc[3][3][3]}; SB;
     SB; hp[7][0]=pk2(gt8_0[0],gt8_0[1]); hp[7][1]=pk2(gt8_1[0],gt8_1[1]); hp[7][2]=pk2(gt8_2[0],gt8_2[1]); hp[7][3]=pk2(gt8_3[0],gt8_3[1]); OPQ(hp[7]); SB;
    *(lds_u32x4_p)(lds+hw+7168)=hp[7];
    _Pragma("unroll") for(int e=0;e<16;e++){ ac2[0][e]=0.f; ac2[1][e]=0.f; }
    BAR();
    s16x8 B24_0=RD(hs0[0]);
    s16x8 B24_1=RD(hs0[1]);
    s16x8 B25_0=RD(b2[0]+32768);
    s16x8 B25_1=RD(b2[1]+32768);
    MF32(ac2[0],Al24_0,Ah24_0,B24_0); s16x8 B26_0=RD(b2[0]+34816); s16x8 B26_1=RD(b2[1]+34816); SB;
    MF32(ac2[1],Al24_1,Ah24_1,B24_0); hs0[0]+=hs0s; s16x8 B27_0=RD(hs0[0]); hs0[1]+=hs0s; s16x8 B27_1=RD(hs0[1]); SB;
    MF32(ac2[0],Al24_2,Ah24_2,B24_1); s16x4 Al33_0=TR(a2[1][0]+32768); s16x4 Ah33_0=TR(a2[1][0]+34816); SB;
    MF32(ac2[1],Al24_3,Ah24_3,B24_1); s16x4 Al33_1=TR(a2[1][1]+32768); SB;
    MF32(ac2[0],Al25_0,Ah25_0,B25_0); s16x4 Ah33_1=TR(a2[1][1]+34816); s16x4 Al33_2=TR(a2[1][0]+33792); SB;
    MF32(ac2[1],Al25_1,Ah25_1,B25_0); s16x4 Ah33_2=TR(a2[1][0]+35840); s16x4 Al33_3=TR(a2[1][1]+33792); SB;
    MF32(ac2[0],Al25_2,Ah25_2,B25_1); s16x4 Ah33_3=TR(a2[1][1]+35840); SB;
    MF32(ac2[1],Al25_3,Ah25_3,B25_1);  SB;
    BAR();
    MF32(ac2[0],Al26_0,Ah26_0,B26_0); s16x8 B28_0=RD(b2[0]+36864); s16x8 B28_1=RD(b2[1]+36864); SB;
    MF32(ac2[1],Al26_1,Ah26_1,B26_0); s16x8 B29_0=RD(b2[0]+38912); s16x8 B29_1=RD(b2[1]+38912); SB;
    MF32(ac2[0],Al26_2,Ah26_2,B26_1); s16x4 Al34_0=TR(a2[1][0]+49152); s16x4 Ah34_0=TR(a2[1][0]+51200); SB;
    MF32(ac2[1],Al26_3,Ah26_3,B26_1); s16x4 Al34_1=TR(a2[1][1]+49152); SB;
    MF32(ac2[0],Al27_0,Ah27_0,B27_0); s16x4 Ah34_1=TR(a2[1][1]+51200); s16x4 Al34_2=TR(a2[1][0]+50176); SB;
    MF32(ac2[1],Al27_1,Ah27_1,B27_0); s16x4 Ah34_2=TR(a2[1][0]+52224); s16x4 Al34_3=TR(a2[1][1]+50176); SB;
    MF32(ac2[0],Al27_2,Ah27_2,B27_1); s16x4 Ah34_3=TR(a2[1][1]+52224); SB;
    MF32(ac2[1],Al27_3,Ah27_3,B27_1);  SB;
    BAR();
    MF32(ac2[0],Al28_0,Ah28_0,B28_0); hs0[0]+=hs0s; s16x8 B30_0=RD(hs0[0]); hs0[1]+=hs0s; s16x8 B30_1=RD(hs0[1]); SB;
    MF32(ac2[1],Al28_1,Ah28_1,B28_0); s16x8 B31_0=RD(b2[0]+40960); s16x8 B31_1=RD(b2[1]+40960); SB;
    MF32(ac2[0],Al28_2,Ah28_2,B28_1); s16x4 Al35_0=TR(a2[2][0]+0); s16x4 Ah35_0=TR(a2[2][0]+2048); SB;
    MF32(ac2[1],Al28_3,Ah28_3,B28_1); s16x4 Al35_1=TR(a2[2][1]+0); SB;
    MF32(ac2[0],Al29_0,Ah29_0,B29_0); s16x4 Ah35_1=TR(a2[2][1]+2048); s16x4 Al35_2=TR(a2[2][0]+1024); SB;
    MF32(ac2[1],Al29_1,Ah29_1,B29_0); s16x4 Ah35_2=TR(a2[2][0]+3072); s16x4 Al35_3=TR(a2[2][1]+1024); SB;
    MF32(ac2[0],Al29_2,Ah29_2,B29_1); s16x4 Ah35_3=TR(a2[2][1]+3072); SB;
    MF32(ac2[1],Al29_3,Ah29_3,B29_1);  SB;
    BAR();
    MF32(ac2[0],Al30_0,Ah30_0,B30_0); s16x8 B32_0=RD(b2[0]+43008); s16x8 B32_1=RD(b2[1]+43008); SB;
    MF32(ac2[1],Al30_1,Ah30_1,B30_0); hs0[0]+=hs0s; s16x8 B33_0=RD(hs0[0]); hs0[1]+=hs0s; s16x8 B33_1=RD(hs0[1]); SB;
    MF32(ac2[0],Al30_2,Ah30_2,B30_1); s16x4 Al36_0=TR(a2[2][0]+16384); s16x4 Ah36_0=TR(a2[2][0]+18432); SB;
    MF32(ac2[1],Al30_3,Ah30_3,B30_1); s16x4 Al36_1=TR(a2[2][1]+16384); SB;
    MF32(ac2[0],Al31_0,Ah31_0,B31_0); s16x4 Ah36_1=TR(a2[2][1]+18432); s16x4 Al36_2=TR(a2[2][0]+17408); SB;
    MF32(ac2[1],Al31_1,Ah31_1,B31_0); s16x4 Ah36_2=TR(a2[2][0]+19456); s16x4 Al36_3=TR(a2[2][1]+17408); SB;
    MF32(ac2[0],Al31_2,Ah31_2,B31_1); s16x4 Ah36_3=TR(a2[2][1]+19456); SB;
    MF32(ac2[1],Al31_3,Ah31_3,B31_1);  SB;
    if(w==0){ unsigned gf=hs_finish(hs_a,hs_r,hs_ack,(gu32*)(ws+WS_DONE)+tile,lane); if(lane==0) *(lds_u32_p)(lds+69636)=gf; } SB;
    BAR();
    MF32(ac2[0],Al32_0,Ah32_0,B32_0); s16x8 B34_0=RD(b2[0]+45056); s16x8 B34_1=RD(b2[1]+45056); SB;
    MF32(ac2[1],Al32_1,Ah32_1,B32_0); s16x8 B35_0=RD(b2[0]+47104); s16x8 B35_1=RD(b2[1]+47104); SB;
    MF32(ac2[0],Al32_2,Ah32_2,B32_1); s16x4 Al37_0=TR(a2[0][0]+0); s16x4 Ah37_0=TR(a2[0][0]+2048); SB;
    MF32(ac2[1],Al32_3,Ah32_3,B32_1); s16x4 Al37_1=TR(a2[0][1]+0); SB;
    MF32(ac2[0],Al33_0,Ah33_0,B33_0); s16x4 Ah37_1=TR(a2[0][1]+2048); s16x4 Al37_2=TR(a2[0][0]+1024); SB;
    MF32(ac2[1],Al33_1,Ah33_1,B33_0); s16x4 Ah37_2=TR(a2[0][0]+3072); s16x4 Al37_3=TR(a2[0][1]+1024); SB;
    MF32(ac2[0],Al33_2,Ah33_2,B33_1); s16x4 Ah37_3=TR(a2[0][1]+3072); SB;
    MF32(ac2[1],Al33_3,Ah33_3,B33_1);  SB;
    BAR();
    MF32(ac2[0],Al34_0,Ah34_0,B34_0); hs0[0]+=hs0s; s16x8 B36_0=RD(hs0[0]); hs0[1]+=hs0s; s16x8 B36_1=RD(hs0[1]); SB;
    MF32(ac2[1],Al34_1,Ah34_1,B34_0); s16x8 B37_0=RD(b2[0]+49152); s16x8 B37_1=RD(b2[1]+49152); SB;
    MF32(ac2[0],Al34_2,Ah34_2,B34_1); s16x4 Al38_0=TR(a2[0][0]+16384); s16x4 Ah38_0=TR(a2[0][0]+18432); SB;
    MF32(ac2[1],Al34_3,Ah34_3,B34_1); s16x4 Al38_1=TR(a2[0][1]+16384); SB;
    MF32(ac2[0],Al35_0,Ah35_0,B35_0); s16x4 Ah38_1=TR(a2[0][1]+18432); s16x4 Al38_2=TR(a2[0][0]+17408); SB;
    MF32(ac2[1],Al35_1,Ah35_1,B35_0); s16x4 Ah38_2=TR(a2[0][0]+19456); s16x4 Al38_3=TR(a2[0][1]+17408); SB;
    MF32(ac2[0],Al35_2,Ah35_2,B35_1); s16x4 Ah38_3=TR(a2[0][1]+19456); SB;
    MF32(ac2[1],Al35_3,Ah35_3,B35_1);  SB;
    BAR();
        MF32(ac2[0],Al36_0,Ah36_0,B36_0); s16x4 Al39_0=TR(a2[1][0]+16384); s16x4 Ah39_0=TR(a2[1][0]+18432); SB;
    MF32(ac2[1],Al36_1,Ah36_1,B36_0); s16x8 B38_0=RD(b2[0]+51200); s16x8 B38_1=RD(b2[1]+51200); SB;
    MF32(ac2[0],Al36_2,Ah36_2,B36_1); hs0[0]+=hs0s; s16x8 B39_0=RD(hs0[0]); hs0[1]+=hs0s; s16x8 B39_1=RD(hs0[1]); SB;
    MF32(ac2[1],Al36_3,Ah36_3,B36_1); s16x4 Al39_1=TR(a2[1][1]+16384); SB;
    MF32(ac2[0],Al37_0,Ah37_0,B37_0); s16x4 Ah39_1=TR(a2[1][1]+18432); s16x4 Al39_2=TR(a2[1][0]+17408); SB;
    MF32(ac2[1],Al37_1,Ah37_1,B37_0); s16x4 Ah39_2=TR(a2[1][0]+19456); s16x4 Al39_3=TR(a2[1][1]+17408); SB;
    MF32(ac2[0],Al37_2,Ah37_2,B37_1); s16x4 Ah39_3=TR(a2[1][1]+19456); SB;
    MF32(ac2[1],Al37_3,Ah37_3,B37_1);  SB;
    BAR();
    MF32(ac2[0],Al38_0,Ah38_0,B38_0); s16x4 Al40_0=TR(a2[1][0]+32768); s16x4 Ah40_0=TR(a2[1][0]+34816); s16x8 B40_0=RD(b2[0]+53248); SB;
    MF32(ac2[1],Al38_1,Ah38_1,B38_0); s16x8 B40_1=RD(b2[1]+53248); s16x8 B41_0=RD(b2[0]+55296); s16x8 B41_1=RD(b2[1]+55296); SB;
    MF32(ac2[0],Al38_2,Ah38_2,B38_1); s16x4 Al40_1=TR(a2[1][1]+32768); s16x4 Ah40_1=TR(a2[1][1]+34816); s16x4 Al40_2=TR(a2[1][0]+33792); SB;
    MF32(ac2[1],Al38_3,Ah38_3,B38_1); s16x4 Ah40_2=TR(a2[1][0]+35840); s16x4 Al40_3=TR(a2[1][1]+33792); s16x4 Ah40_3=TR(a2[1][1]+35840); SB;
    MF32(ac2[0],Al39_0,Ah39_0,B39_0); s16x4 Al41_0=TR(a2[1][0]+49152); s16x4 Ah41_0=TR(a2[1][0]+51200); s16x4 Al41_1=TR(a2[1][1]+49152); SB;
    MF32(ac2[1],Al39_1,Ah39_1,B39_0); s16x4 Ah41_1=TR(a2[1][1]+51200); s16x4 Al41_2=TR(a2[1][0]+50176); s16x4 Ah41_2=TR(a2[1][0]+52224); SB;
    MF32(ac2[0],Al39_2,Ah39_2,B39_1); s16x4 Al41_3=TR(a2[1][1]+50176); s16x4 Ah41_3=TR(a2[1][1]+52224); SB;
    MF32(ac2[1],Al39_3,Ah39_3,B39_1);  SB;
    BAR();
    MF32(ac2[0],Al40_0,Ah40_0,B40_0); s16x4 Al42_0=TR(a2[2][0]+0); s16x4 Ah42_0=TR(a2[2][0]+2048); hs0[0]+=hs0s; s16x8 B42_0=RD(hs0[0]); SB;
    MF32(ac2[1],Al40_1,Ah40_1,B40_0); hs0[1]+=hs0s; s16x8 B42_1=RD(hs0[1]); s16x8 B43_0=RD(b2[0]+57344); s16x8 B43_1=RD(b2[1]+57344); SB;
    MF32(ac2[0],Al40_2,Ah40_2,B40_1); s16x4 Al42_1=TR(a2[2][1]+0); s16x4 Ah42_1=TR(a2[2][1]+2048); s16x4 Al42_2=TR(a2[2][0]+1024); SB;
    MF32(ac2[1],Al40_3,Ah40_3,B40_1); s16x4 Ah42_2=TR(a2[2][0]+3072); s16x4 Al42_3=TR(a2[2][1]+1024); s16x4 Ah42_3=TR(a2[2][1]+3072); SB;
    MF32(ac2[0],Al41_0,Ah41_0,B41_0); s16x4 Al43_0=TR(a2[2][0]+16384); s16x4 Ah43_0=TR(a2[2][0]+18432); s16x4 Al43_1=TR(a2[2][1]+16384); SB;
    MF32(ac2[1],Al41_1,Ah41_1,B41_0); s16x4 Ah43_1=TR(a2[2][1]+18432); s16x4 Al43_2=TR(a2[2][0]+17408); s16x4 Ah43_2=TR(a2[2][0]+19456); SB;
    MF32(ac2[0],Al41_2,Ah41_2,B41_1); s16x4 Al43_3=TR(a2[2][1]+17408); s16x4 Ah43_3=TR(a2[2][1]+19456); SB;
    MF32(ac2[1],Al41_3,Ah41_3,B41_1);  SB;
    BAR();
    MF32(ac2[0],Al42_0,Ah42_0,B42_0); s16x4 Al44_0=TR(a2[0][0]+0); s16x4 Ah44_0=TR(a2[0][0]+2048); s16x8 B44_0=RD(b2[0]+59392); SB;
    MF32(ac2[1],Al42_1,Ah42_1,B42_0); s16x8 B44_1=RD(b2[1]+59392); hs0[0]+=hs0s; s16x8 B45_0=RD(hs0[0]); hs0[1]+=hs0s; s16x8 B45_1=RD(hs0[1]); SB;
    MF32(ac2[0],Al42_2,Ah42_2,B42_1); s16x4 Al44_1=TR(a2[0][1]+0); s16x4 Ah44_1=TR(a2[0][1]+2048); s16x4 Al44_2=TR(a2[0][0]+1024); SB;
    MF32(ac2[1],Al42_3,Ah42_3,B42_1); s16x4 Ah44_2=TR(a2[0][0]+3072); s16x4 Al44_3=TR(a2[0][1]+1024); s16x4 Ah44_3=TR(a2[0][1]+3072); SB;
    MF32(ac2[0],Al43_0,Ah43_0,B43_0); s16x4 Al45_0=TR(a2[0][0]+16384); s16x4 Ah45_0=TR(a2[0][0]+18432); s16x4 Al45_1=TR(a2[0][1]+16384); SB;
    MF32(ac2[1],Al43_1,Ah43_1,B43_0); s16x4 Ah45_1=TR(a2[0][1]+18432); s16x4 Al45_2=TR(a2[0][0]+17408); s16x4 Ah45_2=TR(a2[0][0]+19456); SB;
    MF32(ac2[0],Al43_2,Ah43_2,B43_1); s16x4 Al45_3=TR(a2[0][1]+17408); s16x4 Ah45_3=TR(a2[0][1]+19456); SB;
    MF32(ac2[1],Al43_3,Ah43_3,B43_1);  SB;
    BAR();
    MF32(ac2[0],Al44_0,Ah44_0,B44_0); s16x4 Al46_0=TR(a2[1][0]+16384); s16x4 Ah46_0=TR(a2[1][0]+18432); s16x8 B46_0=RD(b2[0]+61440); SB;
    MF32(ac2[1],Al44_1,Ah44_1,B44_0); s16x8 B46_1=RD(b2[1]+61440); s16x8 B47_0=RD(b2[0]+63488); s16x8 B47_1=RD(b2[1]+63488); SB;
    MF32(ac2[0],Al44_2,Ah44_2,B44_1); s16x4 Al46_1=TR(a2[1][1]+16384); s16x4 Ah46_1=TR(a2[1][1]+18432); s16x4 Al46_2=TR(a2[1][0]+17408); SB;
    MF32(ac2[1],Al44_3,Ah44_3,B44_1); s16x4 Ah46_2=TR(a2[1][0]+19456); s16x4 Al46_3=TR(a2[1][1]+17408); s16x4 Ah46_3=TR(a2[1][1]+19456); SB;
    MF32(ac2[0],Al45_0,Ah45_0,B45_0); s16x4 Al47_0=TR(a2[1][0]+32768); s16x4 Ah47_0=TR(a2[1][0]+34816); s16x4 Al47_1=TR(a2[1][1]+32768); SB;
    MF32(ac2[1],Al45_1,Ah45_1,B45_0); s16x4 Ah47_1=TR(a2[1][1]+34816); s16x4 Al47_2=TR(a2[1][0]+33792); s16x4 Ah47_2=TR(a2[1][0]+35840); SB;
    MF32(ac2[0],Al45_2,Ah45_2,B45_1); s16x4 Al47_3=TR(a2[1][1]+33792); s16x4 Ah47_3=TR(a2[1][1]+35840); SB;
    MF32(ac2[1],Al45_3,Ah45_3,B45_1);  SB;
    BAR();
    MF32(ac2[0],Al46_0,Ah46_0,B46_0);  SB;
    MF32(ac2[1],Al46_1,Ah46_1,B46_0);  SB;
    MF32(ac2[0],Al46_2,Ah46_2,B46_1);  SB;
    MF32(ac2[1],Al46_3,Ah46_3,B46_1);  SB;
    MF32(ac2[0],Al47_0,Ah47_0,B47_0);  SB;
    MF32(ac2[1],Al47_1,Ah47_1,B47_0);  SB;
    MF32(ac2[0],Al47_2,Ah47_2,B47_1);  SB;
    MF32(ac2[1],Al47_3,Ah47_3,B47_1);  SB;
    BAR();
        s16x4 Al48_0=TR(a2[1][0]+49152); SB; f32x2 gt9_0=(f32x2){Q4(ac2[0],0)[0],Q4(ac2[0],0)[1]}*(f32x2){Q4(ac2[0],0)[0],Q4(ac2[0],0)[1]}; f32x2 gt9_1=(f32x2){Q4(ac2[0],0)[2],Q4(ac2[0],0)[3]}*(f32x2){Q4(ac2[0],0)[2],Q4(ac2[0],0)[3]}; f32x2 gt9_2=(f32x2){Q4(ac2[0],1)[0],Q4(ac2[0],1)[1]}*(f32x2){Q4(ac2[0],1)[0],Q4(ac2[0],1)[1]}; f32x2 gt9_3=(f32x2){Q4(ac2[0],1)[2],Q4(ac2[0],1)[3]}*(f32x2){Q4(ac2[0],1)[2],Q4(ac2[0],1)[3]}; SB;
     SB; gt9_0=__builtin_elementwise_fma(gt9_0,gc.c2,gc.c1); gt9_1=__builtin_elementwise_fma(gt9_1,gc.c2,gc.c1); gt9_2=__builtin_elementwise_fma(gt9_2,gc.c2,gc.c1); gt9_3=__builtin_elementwise_fma(gt9_3,gc.c2,gc.c1); SB;
     SB; gt9_0=gt9_0*(f32x2){Q4(ac2[0],0)[0],Q4(ac2[0],0)[1]}; gt9_1=gt9_1*(f32x2){Q4(ac2[0],0)[2],Q4(ac2[0],0)[3]}; gt9_2=gt9_2*(f32x2){Q4(ac2[0],1)[0],Q4(ac2[0],1)[1]}; gt9_3=gt9_3*(f32x2){Q4(ac2[0],1)[2],Q4(ac2[0],1)[3]}; SB;
    s16x4 Ah48_0=TR(a2[1][0]+50176); SB; gt9_0[0]=__builtin_amdgcn_exp2f(gt9_0[0]); gt9_0[1]=__builtin_amdgcn_exp2f(gt9_0[1]); SB;
     SB; gt9_1[0]=__builtin_amdgcn_exp2f(gt9_1[0]); gt9_1[1]=__builtin_amdgcn_exp2f(gt9_1[1]); SB;
     SB; gt9_2[0]=__builtin_amdgcn_exp2f(gt9_2[0]); gt9_2[1]=__builtin_amdgcn_exp2f(gt9_2[1]); SB;
    s16x4 Al48_1=TR(a2[1][1]+49152); SB; gt9_3[0]=__builtin_amdgcn_exp2f(gt9_3[0]); gt9_3[1]=__builtin_amdgcn_exp2f(gt9_3[1]); SB;
     SB; gt9_0=gt9_0+gc.one; gt9_1=gt9_1+gc.one; gt9_2=gt9_2+gc.one; gt9_3=gt9_3+gc.one; SB;
     SB; gt9_0[0]=__builtin_amdgcn_rcpf(gt9_0[0]); gt9_0[1]=__builtin_amdgcn_rcpf(gt9_0[1]); SB;
    s16x4 Ah48_1=TR(a2[1][1]+50176); SB; gt9_1[0]=__builtin_amdgcn_rcpf(gt9_1[0]); gt9_1[1]=__builtin_amdgcn_rcpf(gt9_1[1]); SB;
     SB; gt9_2[0]=__builtin_amdgcn_rcpf(gt9_2[0]); gt9_2[1]=__builtin_amdgcn_rcpf(gt9_2[1]); SB;
     SB; gt9_3[0]=__builtin_amdgcn_rcpf(gt9_3[0]); gt9_3[1]=__builtin_amdgcn_rcpf(gt9_3[1]); SB;
     SB; gt9_0=gt9_0*(f32x2){Q4(ac2[0],0)[0],Q4(ac2[0],0)[1]}; gt9_1=gt9_1*(f32x2){Q4(ac2[0],0)[2],Q4(ac2[0],0)[3]}; gt9_2=gt9_2*(f32x2){Q4(ac2[0],1)[0],Q4(ac2[0],1)[1]}; gt9_3=gt9_3*(f32x2){Q4(ac2[0],1)[2],Q4(ac2[0],1)[3]}; SB;
     SB; hp[0][0]=pk2(gt9_0[0],gt9_0[1]); hp[0][1]=pk2(gt9_1[0],gt9_1[1]); hp[0][2]=pk2(gt9_2[0],gt9_2[1]); hp[0][3]=pk2(gt9_3[0],gt9_3[1]); OPQ(hp[0]); SB;
    s16x4 Al48_2=TR(a2[1][0]+51200); SB; f32x2 gt10_0=(f32x2){Q4(ac2[0],2)[0],Q4(ac2[0],2)[1]}*(f32x2){Q4(ac2[0],2)[0],Q4(ac2[0],2)[1]}; f32x2 gt10_1=(f32x2){Q4(ac2[0],2)[2],Q4(ac2[0],2)[3]}*(f32x2){Q4(ac2[0],2)[2],Q4(ac2[0],2)[3]}; f32x2 gt10_2=(f32x2){Q4(ac2[0],3)[0],Q4(ac2[0],3)[1]}*(f32x2){Q4(ac2[0],3)[0],Q4(ac2[0],3)[1]}; f32x2 gt10_3=(f32x2){Q4(ac2[0],3)[2],Q4(ac2[0],3)[3]}*(f32x2){Q4(ac2[0],3)[2],Q4(ac2[0],3)[3]}; SB;
     SB; gt10_0=__builtin_elementwise_fma(gt10_0,gc.c2,gc.c1); gt10_1=__builtin_elementwise_fma(gt10_1,gc.c2,gc.c1); gt10_2=__builtin_elementwise_fma(gt10_2,gc.c2,gc.c1); gt10_3=__builtin_elementwise_fma(gt10_3,gc.c2,gc.c1); SB;
     SB; gt10_0=gt10_0*(f32x2){Q4(ac2[0],2)[0],Q4(ac2[0],2)[1]}; gt10_1=gt10_1*(f32x2){Q4(ac2[0],2)[2],Q4(ac2[0],2)[3]}; gt10_2=gt10_2*(f32x2){Q4(ac2[0],3)[0],Q4(ac2[0],3)[1]}; gt10_3=gt10_3*(f32x2){Q4(ac2[0],3)[2],Q4(ac2[0],3)[3]}; SB;
    s16x4 Ah48_2=TR(a2[1][0]+52224); SB; gt10_0[0]=__builtin_amdgcn_exp2f(gt10_0[0]); gt10_0[1]=__builtin_amdgcn_exp2f(gt10_0[1]); SB;
     SB; gt10_1[0]=__builtin_amdgcn_exp2f(gt10_1[0]); gt10_1[1]=__builtin_amdgcn_exp2f(gt10_1[1]); SB;
     SB; gt10_2[0]=__builtin_amdgcn_exp2f(gt10_2[0]); gt10_2[1]=__builtin_amdgcn_exp2f(gt10_2[1]); SB;
    s16x4 Al48_3=TR(a2[1][1]+51200); SB; gt10_3[0]=__builtin_amdgcn_exp2f(gt10_3[0]); gt10_3[1]=__builtin_amdgcn_exp2f(gt10_3[1]); SB;
     SB; gt10_0=gt10_0+gc.one; gt10_1=gt10_1+gc.one; gt10_2=gt10_2+gc.one; gt10_3=gt10_3+gc.one; SB;
     SB; gt10_0[0]=__builtin_amdgcn_rcpf(gt10_0[0]); gt10_0[1]=__builtin_amdgcn_rcpf(gt10_0[1]); SB;
    s16x4 Ah48_3=TR(a2[1][1]+52224); SB; gt10_1[0]=__builtin_amdgcn_rcpf(gt10_1[0]); gt10_1[1]=__builtin_amdgcn_rcpf(gt10_1[1]); SB;
     SB; gt10_2[0]=__builtin_amdgcn_rcpf(gt10_2[0]); gt10_2[1]=__builtin_amdgcn_rcpf(gt10_2[1]); SB;
     SB; gt10_3[0]=__builtin_amdgcn_rcpf(gt10_3[0]); gt10_3[1]=__builtin_amdgcn_rcpf(gt10_3[1]); SB;
     SB; gt10_0=gt10_0*(f32x2){Q4(ac2[0],2)[0],Q4(ac2[0],2)[1]}; gt10_1=gt10_1*(f32x2){Q4(ac2[0],2)[2],Q4(ac2[0],2)[3]}; gt10_2=gt10_2*(f32x2){Q4(ac2[0],3)[0],Q4(ac2[0],3)[1]}; gt10_3=gt10_3*(f32x2){Q4(ac2[0],3)[2],Q4(ac2[0],3)[3]}; SB;
     SB; hp[1][0]=pk2(gt10_0[0],gt10_0[1]); hp[1][1]=pk2(gt10_1[0],gt10_1[1]); hp[1][2]=pk2(gt10_2[0],gt10_2[1]); hp[1][3]=pk2(gt10_3[0],gt10_3[1]); OPQ(hp[1]); SB;
    BAR();
        const int fwo=32768+w*4096;
    s16x4 Al49_0=TR(a2[2][0]+0); SB; f32x2 gt11_0=(f32x2){Q4(ac2[1],0)[0],Q4(ac2[1],0)[1]}*(f32x2){Q4(ac2[1],0)[0],Q4(ac2[1],0)[1]}; f32x2 gt11_1=(f32x2){Q4(ac2[1],0)[2],Q4(ac2[1],0)[3]}*(f32x2){Q4(ac2[1],0)[2],Q4(ac2[1],0)[3]}; f32x2 gt11_2=(f32x2){Q4(ac2[1],1)[0],Q4(ac2[1],1)[1]}*(f32x2){Q4(ac2[1],1)[0],Q4(ac2[1],1)[1]}; f32x2 gt11_3=(f32x2){Q4(ac2[1],1)[2],Q4(ac2[1],1)[3]}*(f32x2){Q4(ac2[1],1)[2],Q4(ac2[1],1)[3]}; SB;
    s16x4 Ah49_0=TR(a2[2][0]+1024); SB; gt11_0=__builtin_elementwise_fma(gt11_0,gc.c2,gc.c1); gt11_1=__builtin_elementwise_fma(gt11_1,gc.c2,gc.c1); gt11_2=__builtin_elementwise_fma(gt11_2,gc.c2,gc.c1); gt11_3=__builtin_elementwise_fma(gt11_3,gc.c2,gc.c1); SB;
    s16x4 Al49_1=TR(a2[2][1]+0); SB; gt11_0=gt11_0*(f32x2){Q4(ac2[1],0)[0],Q4(ac2[1],0)[1]}; gt11_1=gt11_1*(f32x2){Q4(ac2[1],0)[2],Q4(ac2[1],0)[3]}; gt11_2=gt11_2*(f32x2){Q4(ac2[1],1)[0],Q4(ac2[1],1)[1]}; gt11_3=gt11_3*(f32x2){Q4(ac2[1],1)[2],Q4(ac2[1],1)[3]}; SB;
     SB; gt11_0[0]=__builtin_amdgcn_exp2f(gt11_0[0]); gt11_0[1]=__builtin_amdgcn_exp2f(gt11_0[1]); SB;
    s16x4 Ah49_1=TR(a2[2][1]+1024); SB; gt11_1[0]=__builtin_amdgcn_exp2f(gt11_1[0]); gt11_1[1]=__builtin_amdgcn_exp2f(gt11_1[1]); SB;
    s16x4 Al49_2=TR(a2[2][0]+2048); SB; gt11_2[0]=__builtin_amdgcn_exp2f(gt11_2[0]); gt11_2[1]=__builtin_amdgcn_exp2f(gt11_2[1]); SB;
     SB; gt11_3[0]=__builtin_amdgcn_exp2f(gt11_3[0]); gt11_3[1]=__builtin_amdgcn_exp2f(gt11_3[1]); SB;
    s16x4 Ah49_2=TR(a2[2][0]+3072); SB; gt11_0=gt11_0+gc.one; gt11_1=gt11_1+gc.one; gt11_2=gt11_2+gc.one; gt11_3=gt11_3+gc.one; SB;
    s16x4 Al49_3=TR(a2[2][1]+2048); SB; gt11_0[0]=__builtin_amdgcn_rcpf(gt11_0[0]); gt11_0[1]=__builtin_amdgcn_rcpf(gt11_0[1]); SB;
     SB; gt11_1[0]=__builtin_amdgcn_rcpf(gt11_1[0]); gt11_1[1]=__builtin_amdgcn_rcpf(gt11_1[1]); SB;
    s16x4 Ah49_3=TR(a2[2][1]+3072); SB; gt11_2[0]=__builtin_amdgcn_rcpf(gt11_2[0]); gt11_2[1]=__builtin_amdgcn_rcpf(gt11_2[1]); SB;
    *(lds_u32x4_p)(lds+b2[0]+fwo+0)=hp[0]; SB; gt11_3[0]=__builtin_amdgcn_rcpf(gt11_3[0]); gt11_3[1]=__builtin_amdgcn_rcpf(gt11_3[1]); SB;
     SB; gt11_0=gt11_0*(f32x2){Q4(ac2[1],0)[0],Q4(ac2[1],0)[1]}; gt11_1=gt11_1*(f32x2){Q4(ac2[1],0)[2],Q4(ac2[1],0)[3]}; gt11_2=gt11_2*(f32x2){Q4(ac2[1],1)[0],Q4(ac2[1],1)[1]}; gt11_3=gt11_3*(f32x2){Q4(ac2[1],1)[2],Q4(ac2[1],1)[3]}; SB;
     SB; hp[2][0]=pk2(gt11_0[0],gt11_0[1]); hp[2][1]=pk2(gt11_1[0],gt11_1[1]); hp[2][2]=pk2(gt11_2[0],gt11_2[1]); hp[2][3]=pk2(gt11_3[0],gt11_3[1]); OPQ(hp[2]); SB;
    *(lds_u32x4_p)(lds+b2[1]+fwo+0)=hp[1]; SB; f32x2 gt12_0=(f32x2){Q4(ac2[1],2)[0],Q4(ac2[1],2)[1]}*(f32x2){Q4(ac2[1],2)[0],Q4(ac2[1],2)[1]}; f32x2 gt12_1=(f32x2){Q4(ac2[1],2)[2],Q4(ac2[1],2)[3]}*(f32x2){Q4(ac2[1],2)[2],Q4(ac2[1],2)[3]}; f32x2 gt12_2=(f32x2){Q4(ac2[1],3)[0],Q4(ac2[1],3)[1]}*(f32x2){Q4(ac2[1],3)[0],Q4(ac2[1],3)[1]}; f32x2 gt12_3=(f32x2){Q4(ac2[1],3)[2],Q4(ac2[1],3)[3]}*(f32x2){Q4(ac2[1],3)[2],Q4(ac2[1],3)[3]}; SB;
     SB; gt12_0=__builtin_elementwise_fma(gt12_0,gc.c2,gc.c1); gt12_1=__builtin_elementwise_fma(gt12_1,gc.c2,gc.c1); gt12_2=__builtin_elementwise_fma(gt12_2,gc.c2,gc.c1); gt12_3=__builtin_elementwise_fma(gt12_3,gc.c2,gc.c1); SB;
     SB; gt12_0=gt12_0*(f32x2){Q4(ac2[1],2)[0],Q4(ac2[1],2)[1]}; gt12_1=gt12_1*(f32x2){Q4(ac2[1],2)[2],Q4(ac2[1],2)[3]}; gt12_2=gt12_2*(f32x2){Q4(ac2[1],3)[0],Q4(ac2[1],3)[1]}; gt12_3=gt12_3*(f32x2){Q4(ac2[1],3)[2],Q4(ac2[1],3)[3]}; SB;
     SB; gt12_0[0]=__builtin_amdgcn_exp2f(gt12_0[0]); gt12_0[1]=__builtin_amdgcn_exp2f(gt12_0[1]); SB;
     SB; gt12_1[0]=__builtin_amdgcn_exp2f(gt12_1[0]); gt12_1[1]=__builtin_amdgcn_exp2f(gt12_1[1]); SB;
     SB; gt12_2[0]=__builtin_amdgcn_exp2f(gt12_2[0]); gt12_2[1]=__builtin_amdgcn_exp2f(gt12_2[1]); SB;
    *(lds_u32x4_p)(lds+b2[0]+fwo+2048)=hp[2]; SB; gt12_3[0]=__builtin_amdgcn_exp2f(gt12_3[0]); gt12_3[1]=__builtin_amdgcn_exp2f(gt12_3[1]); SB;
     SB; gt12_0=gt12_0+gc.one; gt12_1=gt12_1+gc.one; gt12_2=gt12_2+gc.one; gt12_3=gt12_3+gc.one; SB;
     SB; gt12_0[0]=__builtin_amdgcn_rcpf(gt12_0[0]); gt12_0[1]=__builtin_amdgcn_rcpf(gt12_0[1]); SB;
     SB; gt12_1[0]=__builtin_amdgcn_rcpf(gt12_1[0]); gt12_1[1]=__builtin_amdgcn_rcpf(gt12_1[1]); SB;
     SB; gt12_2[0]=__builtin_amdgcn_rcpf(gt12_2[0]); gt12_2[1]=__builtin_amdgcn_rcpf(gt12_2[1]); SB;
     SB; gt12_3[0]=__builtin_amdgcn_rcpf(gt12_3[0]); gt12_3[1]=__builtin_amdgcn_rcpf(gt12_3[1]); SB;
     SB; gt12_0=gt12_0*(f32x2){Q4(ac2[1],2)[0],Q4(ac2[1],2)[1]}; gt12_1=gt12_1*(f32x2){Q4(ac2[1],2)[2],Q4(ac2[1],2)[3]}; gt12_2=gt12_2*(f32x2){Q4(ac2[1],3)[0],Q4(ac2[1],3)[1]}; gt12_3=gt12_3*(f32x2){Q4(ac2[1],3)[2],Q4(ac2[1],3)[3]}; SB;
     SB; hp[3][0]=pk2(gt12_0[0],gt12_0[1]); hp[3][1]=pk2(gt12_1[0],gt12_1[1]); hp[3][2]=pk2(gt12_2[0],gt12_2[1]); hp[3][3]=pk2(gt12_3[0],gt12_3[1]); OPQ(hp[3]); SB;
    *(lds_u32x4_p)(lds+b2[1]+fwo+2048)=hp[3];
    _Pragma("unroll") for(int e=0;e<16;e++){ ac2[0][e]=0.f; ac2[1][e]=0.f; }
    BAR();
    WAITVM(0);
    s16x8 B48_0=RD(b2[0]+32768);
    s16x8 B48_1=RD(b2[1]+32768);
    s16x8 B49_0=RD(b2[0]+34816);
    s16x8 B49_1=RD(b2[1]+34816);
    MF32(ac2[0],Al48_0,Ah48_0,B48_0); s16x4 Al50_0=TR(a2[2][0]+16384); s16x4 Ah50_0=TR(a2[2][0]+17408); s16x8 B50_0=RD(b2[0]+36864); SB;
    MF32(ac2[1],Al48_1,Ah48_1,B48_0); s16x8 B50_1=RD(b2[1]+36864); s16x8 B51_0=RD(b2[0]+38912); s16x8 B51_1=RD(b2[1]+38912); SB;
    MF32(ac2[0],Al48_2,Ah48_2,B48_1); s16x4 Al50_1=TR(a2[2][1]+16384); s16x4 Ah50_1=TR(a2[2][1]+17408); s16x4 Al50_2=TR(a2[2][0]+18432); SB;
    MF32(ac2[1],Al48_3,Ah48_3,B48_1); s16x4 Ah50_2=TR(a2[2][0]+19456); s16x4 Al50_3=TR(a2[2][1]+18432); s16x4 Ah50_3=TR(a2[2][1]+19456); SB;
    MF32(ac2[0],Al49_0,Ah49_0,B49_0); s16x4 Al51_0=TR(a2[0][0]+0); s16x4 Ah51_0=TR(a2[0][0]+1024); s16x4 Al51_1=TR(a2[0][1]+0); SB;
    MF32(ac2[1],Al49_1,Ah49_1,B49_0); s16x4 Ah51_1=TR(a2[0][1]+1024); s16x4 Al51_2=TR(a2[0][0]+2048); s16x4 Ah51_2=TR(a2[0][0]+3072); SB;
    MF32(ac2[0],Al49_2,Ah49_2,B49_1); s16x4 Al51_3=TR(a2[0][1]+2048); s16x4 Ah51_3=TR(a2[0][1]+3072); SB;
    MF32(ac2[1],Al49_3,Ah49_3,B49_1);  SB;
    BAR();
    MF32(ac2[0],Al50_0,Ah50_0,B50_0); s16x4 Al52_0=TR(a2[0][0]+16384); s16x4 Ah52_0=TR(a2[0][0]+17408); s16x8 B52_0=RD(b2[0]+40960); SB;
    MF32(ac2[1],Al50_1,Ah50_1,B50_0); s16x8 B52_1=RD(b2[1]+40960); s16x8 B53_0=RD(b2[0]+43008); s16x8 B53_1=RD(b2[1]+43008); SB;
    MF32(ac2[0],Al50_2,Ah50_2,B50_1); s16x4 Al52_1=TR(a2[0][1]+16384); s16x4 Ah52_1=TR(a2[0][1]+17408); s16x4 Al52_2=TR(a2[0][0]+18432); SB;
    MF32(ac2[1],Al50_3,Ah50_3,B50_1); s16x4 Ah52_2=TR(a2[0][0]+19456); s16x4 Al52_3=TR(a2[0][1]+18432); s16x4 Ah52_3=TR(a2[0][1]+19456); SB;
    MF32(ac2[0],Al51_0,Ah51_0,B51_0); s16x4 Al53_0=TR(a2[1][0]+16384); s16x4 Ah53_0=TR(a2[1][0]+17408); s16x4 Al53_1=TR(a2[1][1]+16384); SB;
    MF32(ac2[1],Al51_1,Ah51_1,B51_0); s16x4 Ah53_1=TR(a2[1][1]+17408); s16x4 Al53_2=TR(a2[1][0]+18432); s16x4 Ah53_2=TR(a2[1][0]+19456); SB;
    MF32(ac2[0],Al51_2,Ah51_2,B51_1); s16x4 Al53_3=TR(a2[1][1]+18432); s16x4 Ah53_3=TR(a2[1][1]+19456); SB;
    MF32(ac2[1],Al51_3,Ah51_3,B51_1);  SB;
    BAR();
    MF32(ac2[0],Al52_0,Ah52_0,B52_0); s16x4 Al54_0=TR(a2[1][0]+32768); s16x4 Ah54_0=TR(a2[1][0]+33792); s16x8 B54_0=RD(b2[0]+45056); SB;
    MF32(ac2[1],Al52_1,Ah52_1,B52_0); s16x8 B54_1=RD(b2[1]+45056); s16x8 B55_0=RD(b2[0]+47104); s16x8 B55_1=RD(b2[1]+47104); SB;
    MF32(ac2[0],Al52_2,Ah52_2,B52_1); s16x4 Al54_1=TR(a2[1][1]+32768); s16x4 Ah54_1=TR(a2[1][1]+33792); s16x4 Al54_2=TR(a2[1][0]+34816); SB;
    MF32(ac2[1],Al52_3,Ah52_3,B52_1); s16x4 Ah54_2=TR(a2[1][0]+35840); s16x4 Al54_3=TR(a2[1][1]+34816); s16x4 Ah54_3=TR(a2[1][1]+35840); SB;
    MF32(ac2[0],Al53_0,Ah53_0,B53_0); s16x4 Al55_0=TR(a2[1][0]+49152); s16x4 Ah55_0=TR(a2[1][0]+50176); s16x4 Al55_1=TR(a2[1][1]+49152); SB;
    MF32(ac2[1],Al53_1,Ah53_1,B53_0); s16x4 Ah55_1=TR(a2[1][1]+50176); s16x4 Al55_2=TR(a2[1][0]+51200); s16x4 Ah55_2=TR(a2[1][0]+52224); SB;
    MF32(ac2[0],Al53_2,Ah53_2,B53_1); s16x4 Al55_3=TR(a2[1][1]+51200); s16x4 Ah55_3=TR(a2[1][1]+52224); SB;
    MF32(ac2[1],Al53_3,Ah53_3,B53_1);  SB;
    BAR();
    MF32(ac2[0],Al54_0,Ah54_0,B54_0);  SB;
    MF32(ac2[1],Al54_1,Ah54_1,B54_0);  SB;
    MF32(ac2[0],Al54_2,Ah54_2,B54_1);  SB;
    MF32(ac2[1],Al54_3,Ah54_3,B54_1);  SB;
    MF32(ac2[0],Al55_0,Ah55_0,B55_0);  SB;
    MF32(ac2[1],Al55_1,Ah55_1,B55_0);  SB;
    MF32(ac2[0],Al55_2,Ah55_2,B55_1);  SB;
    MF32(ac2[1],Al55_3,Ah55_3,B55_1);  SB;
    BAR();
        { int rw=32768+w*8192+h32*512+t32*16; OPQ(rw);
      acc[0][0]=Q4(ac2[0],0)*(-TWO_LOG2E); OPQ(acc[0][0]); acc[0][1]=Q4(ac2[0],1)*(-TWO_LOG2E); OPQ(acc[0][1]); SB;
      _Pragma("unroll") for(int r=0;r<2;r++) ep[0][r]=__builtin_amdgcn_exp2f(acc[0][0][r]); SB; _Pragma("unroll") for(int r=2;r<4;r++) ep[0][r]=__builtin_amdgcn_exp2f(acc[0][0][r]); OPQ(ep[0]); SB; acc[1][0]=Q4(ac2[0],2)*(-TWO_LOG2E); OPQ(acc[1][0]); efm=max3abs(efm,acc[0][0][0],acc[0][0][1]); efm=max3abs(efm,acc[0][0][2],acc[0][0][3]); OPQ(efm); SB;
      _Pragma("unroll") for(int r=0;r<2;r++) ep[1][r]=__builtin_amdgcn_exp2f(acc[0][1][r]); SB; *(lds_f32x4_p)(lds+rw+0)=ep[0]; SB; _Pragma("unroll") for(int r=2;r<4;r++) ep[1][r]=__builtin_amdgcn_exp2f(acc[0][1][r]); OPQ(ep[1]); SB; acc[1][1]=Q4(ac2[0],3)*(-TWO_LOG2E); OPQ(acc[1][1]); efm=max3abs(efm,acc[0][1][0],acc[0][1][1]); efm=max3abs(efm,acc[0][1][2],acc[0][1][3]); OPQ(efm); SB;
      _Pragma("unroll") for(int r=0;r<2;r++) ep[2][r]=__builtin_amdgcn_exp2f(acc[1][0][r]); SB; *(lds_f32x4_p)(lds+rw+1024)=ep[1]; SB; _Pragma("unroll") for(int r=2;r<4;r++) ep[2][r]=__builtin_amdgcn_exp2f(acc[1][0][r]); OPQ(ep[2]); SB; acc[2][0]=Q4(ac2[1],0)*(-TWO_LOG2E); OPQ(acc[2][0]); efm=max3abs(efm,acc[1][0][0],acc[1][0][1]); efm=max3abs(efm,acc[1][0][2],acc[1][0][3]); OPQ(efm); SB;
      _Pragma("unroll") for(int r=0;r<2;r++) ep[3][r]=__builtin_amdgcn_exp2f(acc[1][1][r]); SB; *(lds_f32x4_p)(lds+rw+2048)=ep[2]; SB; _Pragma("unroll") for(int r=2;r<4;r++) ep[3][r]=__builtin_amdgcn_exp2f(acc[1][1][r]); OPQ(ep[3]); SB; acc[2][1]=Q4(ac2[1],1)*(-TWO_LOG2E); OPQ(acc[2][1]); efm=max3abs(efm,acc[1][1][0],acc[1][1][1]); efm=max3abs(efm,acc[1][1][2],acc[1][1][3]); OPQ(efm); SB;
      _Pragma("unroll") for(int r=0;r<2;r++) ep[4][r]=__builtin_amdgcn_exp2f(acc[2][0][r]); SB; *(lds_f32x4_p)(lds+rw+3072)=ep[3]; SB; _Pragma("unroll") for(int r=2;r<4;r++) ep[4][r]=__builtin_amdgcn_exp2f(acc[2][0][r]); OPQ(ep[4]); SB; acc[3][0]=Q4(ac2[1],2)*(-TWO_LOG2E); OPQ(acc[3][0]); efm=max3abs(efm,acc[2][0][0],acc[2][0][1]); efm=max3abs(efm,acc[2][0][2],acc[2][0][3]); OPQ(efm); SB;
      _Pragma("unroll") for(int r=0;r<2;r++) ep[5][r]=__builtin_amdgcn_exp2f(acc[2][1][r]); SB; *(lds_f32x4_p)(lds+rw+4096)=ep[4]; SB; _Pragma("unroll") for(int r=2;r<4;r++) ep[5][r]=__builtin_amdgcn_exp2f(acc[2][1][r]); OPQ(ep[5]); SB; acc[3][1]=Q4(ac2[1],3)*(-TWO_LOG2E); OPQ(acc[3][1]); efm=max3abs(efm,acc[2][1][0],acc[2][1][1]); efm=max3abs(efm,acc[2][1][2],acc[2][1][3]); OPQ(efm); SB;
      _Pragma("unroll") for(int r=0;r<2;r++) ep[6][r]=__builtin_amdgcn_exp2f(acc[3][0][r]); SB; *(lds_f32x4_p)(lds+rw+5120)=ep[5]; SB; _Pragma("unroll") for(int r=2;r<4;r++) ep[6][r]=__builtin_amdgcn_exp2f(acc[3][0][r]); OPQ(ep[6]); SB; efm=max3abs(efm,acc[3][0][0],acc[3][0][1]); efm=max3abs(efm,acc[3][0][2],acc[3][0][3]); OPQ(efm); SB;
      _Pragma("unroll") for(int r=0;r<2;r++) ep[7][r]=__builtin_amdgcn_exp2f(acc[3][1][r]); SB; *(lds_f32x4_p)(lds+rw+6144)=ep[6]; SB; _Pragma("unroll") for(int r=2;r<4;r++) ep[7][r]=__builtin_amdgcn_exp2f(acc[3][1][r]); OPQ(ep[7]); SB; efm=max3abs(efm,acc[3][1][0],acc[3][1][1]); efm=max3abs(efm,acc[3][1][2],acc[3][1][3]); OPQ(efm); SB;
      *(lds_f32x4_p)(lds+rw+7168)=ep[7]; SB;
      if(efm>EXP_LIM) *(lds_u32_p)(lds+69632)=1u; }
        asm volatile(".Lend1:");
  }
  BAR();

  {
    const int u=wid, t=lane&31, h=lane>>5;
    FPMODE();
    unsigned slowf=*(volatile LAS unsigned*)(lds+69632);
    float eg[8];
    { int ga=73728+u*1024+(16*((lane&15)>>3)+8*h+(lane&7))*4;
      _Pragma("unroll") for(int n=0;n<8;n++) eg[n]=*(LAS float*)(lds+ga+n*128); }
    int rr=32768+h*1024+t*16; OPQ(rr);
    const int a_=(lane&15)>>2, p_=lane&3, bl=(lane>>4)&1;
    int wa[2][3];
    _Pragma("unroll") for(int m=0;m<2;m++){ int z=(8*h+a_)*128+(((2*(p_&1)+m)^((a_>>1)&1))*32)+(2*bl+(p_>>1))*8; wa[m][0]=z; wa[m][1]=z+65536; wa[m][2]=z+131072; OPQ(wa[m][0]); OPQ(wa[m][1]); OPQ(wa[m][2]); }
    auto r_out=__builtin_amdgcn_make_buffer_rsrc(outp+((size_t)(b*4000+t0))*4096, 0, 32*4096, 0x00020000);
    int vo=(t>>2)*16384+h*64+(lane&3)*16+u*512; OPQ(vo);
    i32x4 r_wo=mkrsrc(wo,256*512); int voffWo=(lane>>3)*512+(((lane&7)^(((lane>>4)&1)<<1))*16);
    const bool slow=__builtin_amdgcn_readfirstlane(slowf)!=0u;
    u32x2 Ja[16], Jb[16];
    f32x16 o0, o1;
    const f32x16 z16={0.f,0.f,0.f,0.f,0.f,0.f,0.f,0.f,0.f,0.f,0.f,0.f,0.f,0.f,0.f,0.f};
    #define JF(k) BC(__builtin_shufflevector(Ja[k],Jb[k],0,1,2,3))
    SB;

    s16x4 Wl2_0, Wh2_0;
    s16x4 Wl2_1, Wh2_1;
    s16x4 Wl2_2, Wh2_2;
    s16x4 Wl2_3, Wh2_3;
    s16x4 Wl2_4, Wh2_4;
    if(wid>=4 && !slow){
            f32x4 Ra0a=*(lds_f32x4_p)(lds+rr+0); f32x4 Rb0a=*(lds_f32x4_p)(lds+rr+512);
      f32x4 Ra1a=*(lds_f32x4_p)(lds+rr+2048); f32x4 Rb1a=*(lds_f32x4_p)(lds+rr+2560);
      f32x4 Ra2a=*(lds_f32x4_p)(lds+rr+4096); f32x4 Rb2a=*(lds_f32x4_p)(lds+rr+4608);
      s16x4 Wl0_0a=TR(wa[0][2]+0); s16x4 Wh0_0a=TR(wa[0][2]+512); s16x4 Wl1_0a=TR(wa[1][2]+0); s16x4 Wh1_0a=TR(wa[1][2]+512);
      _Pragma("unroll") for(int n=0;n<8;n++){ eg[n]=__builtin_amdgcn_exp2f(eg[n]); OPQ(eg[n]); }
      SB; asm volatile("s_nop 1"); SB; Ja[0]=jhalf<0>(eg[0],Ra0a); SB; Jb[0]=jhalf<4>(eg[0],Rb0a); SB;
      o0=__builtin_amdgcn_mfma_f32_32x32x16_bf16(CAT(Wl0_0a,Wh0_0a),JF(0),z16,0,0,0); OPQ(o0); s16x4 Wl0_1a=TR(wa[0][2]+2048); s16x4 Wh0_1a=TR(wa[0][2]+2560); f32x4 Ra3a=*(lds_f32x4_p)(lds+rr+6144); SB; Ja[1]=jhalf<8>(eg[0],Ra1a); SB;
      o1=__builtin_amdgcn_mfma_f32_32x32x16_bf16(CAT(Wl1_0a,Wh1_0a),JF(0),z16,0,0,0); OPQ(o1); s16x4 Wl1_1a=TR(wa[1][2]+2048); s16x4 Wh1_1a=TR(wa[1][2]+2560); f32x4 Rb3a=*(lds_f32x4_p)(lds+rr+6656); SB; Jb[1]=jhalf<12>(eg[0],Rb1a); SB;
      LBAR(); SB;
      o0=__builtin_amdgcn_mfma_f32_32x32x16_bf16(CAT(Wl0_1a,Wh0_1a),JF(1),o0,0,0,0); OPQ(o0); s16x4 Wl0_2a=TR(wa[0][2]+4096); s16x4 Wh0_2a=TR(wa[0][2]+4608); f32x4 Ra4a=*(lds_f32x4_p)(lds+rr+8192); SB; Ja[2]=jhalf<0>(eg[1],Ra2a); SB;
      o1=__builtin_amdgcn_mfma_f32_32x32x16_bf16(CAT(Wl1_1a,Wh1_1a),JF(1),o1,0,0,0); OPQ(o1); s16x4 Wl1_2a=TR(wa[1][2]+4096); s16x4 Wh1_2a=TR(wa[1][2]+4608); f32x4 Rb4a=*(lds_f32x4_p)(lds+rr+8704); SB; Jb[2]=jhalf<4>(eg[1],Rb2a); SB;
      o0=__builtin_amdgcn_mfma_f32_32x32x16_bf16(CAT(Wl0_2a,Wh0_2a),JF(2),o0,0,0,0); OPQ(o0); s16x4 Wl0_3a=TR(wa[0][2]+6144); s16x4 Wh0_3a=TR(wa[0][2]+6656); f32x4 Ra5a=*(lds_f32x4_p)(lds+rr+10240); SB; Ja[3]=jhalf<8>(eg[1],Ra3a); SB;
      o1=__builtin_amdgcn_mfma_f32_32x32x16_bf16(CAT(Wl1_2a,Wh1_2a),JF(2),o1,0,0,0); OPQ(o1); s16x4 Wl1_3a=TR(wa[1][2]+6144); s16x4 Wh1_3a=TR(wa[1][2]+6656); f32x4 Rb5a=*(lds_f32x4_p)(lds+rr+10752); SB; Jb[3]=jhalf<12>(eg[1],Rb3a); SB;
      o0=__builtin_amdgcn_mfma_f32_32x32x16_bf16(CAT(Wl0_3a,Wh0_3a),JF(3),o0,0,0,0); OPQ(o0); s16x4 Wl0_4a=TR(wa[0][2]+8192); s16x4 Wh0_4a=TR(wa[0][2]+8704); f32x4 Ra6a=*(lds_f32x4_p)(lds+rr+12288); SB; Ja[4]=jhalf<0>(eg[2],Ra4a); SB;
      o1=__builtin_amdgcn_mfma_f32_32x32x16_bf16(CAT(Wl1_3a,Wh1_3a),JF(3),o1,0,0,0); OPQ(o1); s16x4 Wl1_4a=TR(wa[1][2]+8192); s16x4 Wh1_4a=TR(wa[1][2]+8704); f32x4 Rb6a=*(lds_f32x4_p)(lds+rr+12800); SB; Jb[4]=jhalf<4>(eg[2],Rb4a); SB;
      o0=__builtin_amdgcn_mfma_f32_32x32x16_bf16(CAT(Wl0_4a,Wh0_4a),JF(4),o0,0,0,0); OPQ(o0); s16x4 Wl0_5a=TR(wa[0][2]+10240); s16x4 Wh0_5a=TR(wa[0][2]+10752); f32x4 Ra7a=*(lds_f32x4_p)(lds+rr+14336); SB; Ja[5]=jhalf<8>(eg[2],Ra5a); SB;
      o1=__builtin_amdgcn_mfma_f32_32x32x16_bf16(CAT(Wl1_4a,Wh1_4a),JF(4),o1,0,0,0); OPQ(o1); s16x4 Wl1_5a=TR(wa[1][2]+10240); s16x4 Wh1_5a=TR(wa[1][2]+10752); f32x4 Rb7a=*(lds_f32x4_p)(lds+rr+14848); SB; Jb[5]=jhalf<12>(eg[2],Rb5a); SB;
      o0=__builtin_amdgcn_mfma_f32_32x32x16_bf16(CAT(Wl0_5a,Wh0_5a),JF(5),o0,0,0,0); OPQ(o0); s16x4 Wl0_6a=TR(wa[0][2]+12288); s16x4 Wh0_6a=TR(wa[0][2]+12800); f32x4 Ra8a=*(lds_f32x4_p)(lds+rr+16384); SB; Ja[6]=jhalf<0>(eg[3],Ra6a); SB;
      o1=__builtin_amdgcn_mfma_f32_32x32x16_bf16(CAT(Wl1_5a,Wh1_5a),JF(5),o1,0,0,0); OPQ(o1); s16x4 Wl1_6a=TR(wa[1][2]+12288); s16x4 Wh1_6a=TR(wa[1][2]+12800); f32x4 Rb8a=*(lds_f32x4_p)(lds+rr+16896); SB; Jb[6]=jhalf<4>(eg[3],Rb6a); SB;
      o0=__builtin_amdgcn_mfma_f32_32x32x16_bf16(CAT(Wl0_6a,Wh0_6a),JF(6),o0,0,0,0); OPQ(o0); s16x4 Wl0_7a=TR(wa[0][2]+14336); s16x4 Wh0_7a=TR(wa[0][2]+14848); f32x4 Ra9a=*(lds_f32x4_p)(lds+rr+18432); SB; Ja[7]=jhalf<8>(eg[3],Ra7a); SB;
      o1=__builtin_amdgcn_mfma_f32_32x32x16_bf16(CAT(Wl1_6a,Wh1_6a),JF(6),o1,0,0,0); OPQ(o1); s16x4 Wl1_7a=TR(wa[1][2]+14336); s16x4 Wh1_7a=TR(wa[1][2]+14848); f32x4 Rb9a=*(lds_f32x4_p)(lds+rr+18944); SB; Jb[7]=jhalf<12>(eg[3],Rb7a); SB;
      o0=__builtin_amdgcn_mfma_f32_32x32x16_bf16(CAT(Wl0_7a,Wh0_7a),JF(7),o0,0,0,0); OPQ(o0); s16x4 Wl0_8a=TR(wa[0][0]+0); s16x4 Wh0_8a=TR(wa[0][0]+512); f32x4 Ra10a=*(lds_f32x4_p)(lds+rr+20480); SB; Ja[8]=jhalf<0>(eg[4],Ra8a); SB;
      o1=__builtin_amdgcn_mfma_f32_32x32x16_bf16(CAT(Wl1_7a,Wh1_7a),JF(7),o1,0,0,0); OPQ(o1); s16x4 Wl1_8a=TR(wa[1][0]+0); s16x4 Wh1_8a=TR(wa[1][0]+512); f32x4 Rb10a=*(lds_f32x4_p)(lds+rr+20992); SB; Jb[8]=jhalf<4>(eg[4],Rb8a); SB;
      WAITVM(0); LBAR(); SB;
      o0=__builtin_amdgcn_mfma_f32_32x32x16_bf16(CAT(Wl0_8a,Wh0_8a),JF(8),o0,0,0,0); OPQ(o0); s16x4 Wl0_9a=TR(wa[0][0]+2048); s16x4 Wh0_9a=TR(wa[0][0]+2560); f32x4 Ra11a=*(lds_f32x4_p)(lds+rr+22528); SB; Ja[9]=jhalf<8>(eg[4],Ra9a); SB;
      o1=__builtin_amdgcn_mfma_f32_32x32x16_bf16(CAT(Wl1_8a,Wh1_8a),JF(8),o1,0,0,0); OPQ(o1); s16x4 Wl1_9a=TR(wa[1][0]+2048); s16x4 Wh1_9a=TR(wa[1][0]+2560); f32x4 Rb11a=*(lds_f32x4_p)(lds+rr+23040); SB; Jb[9]=jhalf<12>(eg[4],Rb9a); SB;
      o0=__builtin_amdgcn_mfma_f32_32x32x16_bf16(CAT(Wl0_9a,Wh0_9a),JF(9),o0,0,0,0); OPQ(o0); s16x4 Wl0_10a=TR(wa[0][0]+4096); s16x4 Wh0_10a=TR(wa[0][0]+4608); f32x4 Ra12a=*(lds_f32x4_p)(lds+rr+24576); SB; Ja[10]=jhalf<0>(eg[5],Ra10a); SB;
      o1=__builtin_amdgcn_mfma_f32_32x32x16_bf16(CAT(Wl1_9a,Wh1_9a),JF(9),o1,0,0,0); OPQ(o1); s16x4 Wl1_10a=TR(wa[1][0]+4096); s16x4 Wh1_10a=TR(wa[1][0]+4608); f32x4 Rb12a=*(lds_f32x4_p)(lds+rr+25088); SB; Jb[10]=jhalf<4>(eg[5],Rb10a); SB;
      o0=__builtin_amdgcn_mfma_f32_32x32x16_bf16(CAT(Wl0_10a,Wh0_10a),JF(10),o0,0,0,0); OPQ(o0); s16x4 Wl0_11a=TR(wa[0][0]+6144); s16x4 Wh0_11a=TR(wa[0][0]+6656); f32x4 Ra13a=*(lds_f32x4_p)(lds+rr+26624); SB; Ja[11]=jhalf<8>(eg[5],Ra11a); SB;
      o1=__builtin_amdgcn_mfma_f32_32x32x16_bf16(CAT(Wl1_10a,Wh1_10a),JF(10),o1,0,0,0); OPQ(o1); s16x4 Wl1_11a=TR(wa[1][0]+6144); s16x4 Wh1_11a=TR(wa[1][0]+6656); f32x4 Rb13a=*(lds_f32x4_p)(lds+rr+27136); SB; Jb[11]=jhalf<12>(eg[5],Rb11a); SB;
      o0=__builtin_amdgcn_mfma_f32_32x32x16_bf16(CAT(Wl0_11a,Wh0_11a),JF(11),o0,0,0,0); OPQ(o0); s16x4 Wl0_12a=TR(wa[0][0]+8192); s16x4 Wh0_12a=TR(wa[0][0]+8704); f32x4 Ra14a=*(lds_f32x4_p)(lds+rr+28672); SB; Ja[12]=jhalf<0>(eg[6],Ra12a); SB;
      o1=__builtin_amdgcn_mfma_f32_32x32x16_bf16(CAT(Wl1_11a,Wh1_11a),JF(11),o1,0,0,0); OPQ(o1); s16x4 Wl1_12a=TR(wa[1][0]+8192); s16x4 Wh1_12a=TR(wa[1][0]+8704); f32x4 Rb14a=*(lds_f32x4_p)(lds+rr+29184); SB; Jb[12]=jhalf<4>(eg[6],Rb12a); SB;
      o0=__builtin_amdgcn_mfma_f32_32x32x16_bf16(CAT(Wl0_12a,Wh0_12a),JF(12),o0,0,0,0); OPQ(o0); s16x4 Wl0_13a=TR(wa[0][0]+10240); s16x4 Wh0_13a=TR(wa[0][0]+10752); f32x4 Ra15a=*(lds_f32x4_p)(lds+rr+30720); SB; Ja[13]=jhalf<8>(eg[6],Ra13a); SB;
      o1=__builtin_amdgcn_mfma_f32_32x32x16_bf16(CAT(Wl1_12a,Wh1_12a),JF(12),o1,0,0,0); OPQ(o1); s16x4 Wl1_13a=TR(wa[1][0]+10240); s16x4 Wh1_13a=TR(wa[1][0]+10752); f32x4 Rb15a=*(lds_f32x4_p)(lds+rr+31232); SB; Jb[13]=jhalf<12>(eg[6],Rb13a); SB;
      o0=__builtin_amdgcn_mfma_f32_32x32x16_bf16(CAT(Wl0_13a,Wh0_13a),JF(13),o0,0,0,0); OPQ(o0); s16x4 Wl0_14a=TR(wa[0][0]+12288); s16x4 Wh0_14a=TR(wa[0][0]+12800); SB; Ja[14]=jhalf<0>(eg[7],Ra14a); SB;
      o1=__builtin_amdgcn_mfma_f32_32x32x16_bf16(CAT(Wl1_13a,Wh1_13a),JF(13),o1,0,0,0); OPQ(o1); s16x4 Wl1_14a=TR(wa[1][0]+12288); s16x4 Wh1_14a=TR(wa[1][0]+12800); SB; Jb[14]=jhalf<4>(eg[7],Rb14a); SB;
      o0=__builtin_amdgcn_mfma_f32_32x32x16_bf16(CAT(Wl0_14a,Wh0_14a),JF(14),o0,0,0,0); OPQ(o0); s16x4 Wl0_15a=TR(wa[0][0]+14336); s16x4 Wh0_15a=TR(wa[0][0]+14848); SB; Ja[15]=jhalf<8>(eg[7],Ra15a); SB;
      o1=__builtin_amdgcn_mfma_f32_32x32x16_bf16(CAT(Wl1_14a,Wh1_14a),JF(14),o1,0,0,0); OPQ(o1); s16x4 Wl1_15a=TR(wa[1][0]+14336); s16x4 Wh1_15a=TR(wa[1][0]+14848); SB; Jb[15]=jhalf<12>(eg[7],Rb15a); SB;
      o0=__builtin_amdgcn_mfma_f32_32x32x16_bf16(CAT(Wl0_15a,Wh0_15a),JF(15),o0,0,0,0); OPQ(o0); Wl2_0=TR(wa[0][2]+16384); Wh2_0=TR(wa[0][2]+16896); Wl2_2=TR(wa[0][2]+20480); Wh2_2=TR(wa[0][2]+20992); Wl2_4=TR(wa[0][2]+24576); Wh2_4=TR(wa[0][2]+25088); SB;  SB;
      o1=__builtin_amdgcn_mfma_f32_32x32x16_bf16(CAT(Wl1_15a,Wh1_15a),JF(15),o1,0,0,0); OPQ(o1); Wl2_1=TR(wa[0][2]+18432); Wh2_1=TR(wa[0][2]+18944); Wl2_3=TR(wa[0][2]+22528); Wh2_3=TR(wa[0][2]+23040); SB;  SB;
      __builtin_amdgcn_s_setprio(2);
    } else if(__builtin_expect(!slow,1)) {
      f32x4 Ra0b=*(lds_f32x4_p)(lds+rr+0); f32x4 Rb0b=*(lds_f32x4_p)(lds+rr+512);
      f32x4 Ra1b=*(lds_f32x4_p)(lds+rr+2048); f32x4 Rb1b=*(lds_f32x4_p)(lds+rr+2560);
      _Pragma("unroll") for(int n=0;n<8;n++){ eg[n]=__builtin_amdgcn_exp2f(eg[n]); OPQ(eg[n]); }
      BAR();
      dma16(r_wo, voffWo, wid*16384 + 256, wid*4096 + 81920);
      dma16(r_wo, voffWo, wid*16384 + 4352, wid*4096 + 82944);
      dma16(r_wo, voffWo, wid*16384 + 8448, wid*4096 + 83968);
      dma16(r_wo, voffWo, wid*16384 + 12544, wid*4096 + 84992);
      dma16(r_wo, voffWo, wid*16384 + 65792, wid*4096 + 98304);
      dma16(r_wo, voffWo, wid*16384 + 69888, wid*4096 + 99328);
      dma16(r_wo, voffWo, wid*16384 + 73984, wid*4096 + 100352);
      dma16(r_wo, voffWo, wid*16384 + 78080, wid*4096 + 101376);
      dma16(r_wo, voffWo, wid*16384 + 384, wid*4096 + 114688);
      dma16(r_wo, voffWo, wid*16384 + 4480, wid*4096 + 115712);
      dma16(r_wo, voffWo, wid*16384 + 8576, wid*4096 + 116736);
      dma16(r_wo, voffWo, wid*16384 + 12672, wid*4096 + 117760);
      dma16(r_wo, voffWo, wid*16384 + 65920, wid*4096 + 65536);
      dma16(r_wo, voffWo, wid*16384 + 70016, wid*4096 + 66560);
      dma16(r_wo, voffWo, wid*16384 + 74112, wid*4096 + 67584);
      dma16(r_wo, voffWo, wid*16384 + 78208, wid*4096 + 68608);
      __builtin_amdgcn_s_setprio(1);
      SB; asm volatile("s_nop 1"); SB;
      f32x4 Ra2b=*(lds_f32x4_p)(lds+rr+4096);  SB; Ja[0]=jhalfs<0>(eg[0],Ra0b); SB; f32x4 Rb2b=*(lds_f32x4_p)(lds+rr+4608); SB; Jb[0]=jhalfs<4>(eg[0],Rb0b); SB;
      f32x4 Ra3b=*(lds_f32x4_p)(lds+rr+6144);  SB; Ja[1]=jhalfs<8>(eg[0],Ra1b); SB; f32x4 Rb3b=*(lds_f32x4_p)(lds+rr+6656); SB; Jb[1]=jhalfs<12>(eg[0],Rb1b); SB;
      WAITVM(0); LBAR(); SB;
      f32x4 Ra4b=*(lds_f32x4_p)(lds+rr+8192);  SB; Ja[2]=jhalfs<0>(eg[1],Ra2b); SB; f32x4 Rb4b=*(lds_f32x4_p)(lds+rr+8704); SB; Jb[2]=jhalfs<4>(eg[1],Rb2b); SB;
      f32x4 Ra5b=*(lds_f32x4_p)(lds+rr+10240);  SB; Ja[3]=jhalfs<8>(eg[1],Ra3b); SB; f32x4 Rb5b=*(lds_f32x4_p)(lds+rr+10752); SB; Jb[3]=jhalfs<12>(eg[1],Rb3b); SB;
      f32x4 Ra6b=*(lds_f32x4_p)(lds+rr+12288);  SB; Ja[4]=jhalfs<0>(eg[2],Ra4b); SB; f32x4 Rb6b=*(lds_f32x4_p)(lds+rr+12800); SB; Jb[4]=jhalfs<4>(eg[2],Rb4b); SB;
      f32x4 Ra7b=*(lds_f32x4_p)(lds+rr+14336);  SB; Ja[5]=jhalfs<8>(eg[2],Ra5b); SB; f32x4 Rb7b=*(lds_f32x4_p)(lds+rr+14848); SB; Jb[5]=jhalfs<12>(eg[2],Rb5b); SB;
      f32x4 Ra8b=*(lds_f32x4_p)(lds+rr+16384);  SB; Ja[6]=jhalfs<0>(eg[3],Ra6b); SB; f32x4 Rb8b=*(lds_f32x4_p)(lds+rr+16896); SB; Jb[6]=jhalfs<4>(eg[3],Rb6b); SB;
      f32x4 Ra9b=*(lds_f32x4_p)(lds+rr+18432);  SB; Ja[7]=jhalfs<8>(eg[3],Ra7b); SB; f32x4 Rb9b=*(lds_f32x4_p)(lds+rr+18944); SB; Jb[7]=jhalfs<12>(eg[3],Rb7b); SB;
      f32x4 Ra10b=*(lds_f32x4_p)(lds+rr+20480);  SB; Ja[8]=jhalfs<0>(eg[4],Ra8b); SB; f32x4 Rb10b=*(lds_f32x4_p)(lds+rr+20992); SB; Jb[8]=jhalfs<4>(eg[4],Rb8b); SB;
      f32x4 Ra11b=*(lds_f32x4_p)(lds+rr+22528);  SB; Ja[9]=jhalfs<8>(eg[4],Ra9b); SB; f32x4 Rb11b=*(lds_f32x4_p)(lds+rr+23040); SB; Jb[9]=jhalfs<12>(eg[4],Rb9b); SB;
      f32x4 Ra12b=*(lds_f32x4_p)(lds+rr+24576);  SB; Ja[10]=jhalfs<0>(eg[5],Ra10b); SB; f32x4 Rb12b=*(lds_f32x4_p)(lds+rr+25088); SB; Jb[10]=jhalfs<4>(eg[5],Rb10b); SB;
      f32x4 Ra13b=*(lds_f32x4_p)(lds+rr+26624);  SB; Ja[11]=jhalfs<8>(eg[5],Ra11b); SB; f32x4 Rb13b=*(lds_f32x4_p)(lds+rr+27136); SB; Jb[11]=jhalfs<12>(eg[5],Rb11b); SB;
      f32x4 Ra14b=*(lds_f32x4_p)(lds+rr+28672);  SB; Ja[12]=jhalfs<0>(eg[6],Ra12b); SB; f32x4 Rb14b=*(lds_f32x4_p)(lds+rr+29184); SB; Jb[12]=jhalfs<4>(eg[6],Rb12b); SB;
      f32x4 Ra15b=*(lds_f32x4_p)(lds+rr+30720);  SB; Ja[13]=jhalfs<8>(eg[6],Ra13b); SB; f32x4 Rb15b=*(lds_f32x4_p)(lds+rr+31232); SB; Jb[13]=jhalfs<12>(eg[6],Rb13b); SB;
        SB; Ja[14]=jhalfs<0>(eg[7],Ra14b); SB;  SB; Jb[14]=jhalfs<4>(eg[7],Rb14b); SB;
       s16x4 Wl0_0b=TR(wa[0][2]+0); s16x4 Wh0_0b=TR(wa[0][2]+512); s16x4 Wl1_0b=TR(wa[1][2]+0); s16x4 Wh1_0b=TR(wa[1][2]+512); s16x4 Wl0_1b=TR(wa[0][2]+2048); s16x4 Wh0_1b=TR(wa[0][2]+2560); s16x4 Wl1_1b=TR(wa[1][2]+2048); s16x4 Wh1_1b=TR(wa[1][2]+2560); s16x4 Wl0_2b=TR(wa[0][2]+4096); s16x4 Wh0_2b=TR(wa[0][2]+4608); SB; Ja[15]=jhalfs<8>(eg[7],Ra15b); SB;  SB; Jb[15]=jhalfs<12>(eg[7],Rb15b); SB;
      __builtin_amdgcn_s_setprio(3);       o0=__builtin_amdgcn_mfma_f32_32x32x16_bf16(CAT(Wl0_0b,Wh0_0b),JF(0),z16,0,0,0); OPQ(o0); s16x4 Wl1_2b=TR(wa[1][2]+4096); s16x4 Wh1_2b=TR(wa[1][2]+4608); SB;
      o1=__builtin_amdgcn_mfma_f32_32x32x16_bf16(CAT(Wl1_0b,Wh1_0b),JF(0),z16,0,0,0); OPQ(o1); s16x4 Wl0_3b=TR(wa[0][2]+6144); s16x4 Wh0_3b=TR(wa[0][2]+6656); SB;
      o0=__builtin_amdgcn_mfma_f32_32x32x16_bf16(CAT(Wl0_1b,Wh0_1b),JF(1),o0,0,0,0); OPQ(o0); s16x4 Wl1_3b=TR(wa[1][2]+6144); s16x4 Wh1_3b=TR(wa[1][2]+6656); SB;
      o1=__builtin_amdgcn_mfma_f32_32x32x16_bf16(CAT(Wl1_1b,Wh1_1b),JF(1),o1,0,0,0); OPQ(o1); s16x4 Wl0_4b=TR(wa[0][2]+8192); s16x4 Wh0_4b=TR(wa[0][2]+8704); SB;
      o0=__builtin_amdgcn_mfma_f32_32x32x16_bf16(CAT(Wl0_2b,Wh0_2b),JF(2),o0,0,0,0); OPQ(o0); s16x4 Wl1_4b=TR(wa[1][2]+8192); s16x4 Wh1_4b=TR(wa[1][2]+8704); SB;
      o1=__builtin_amdgcn_mfma_f32_32x32x16_bf16(CAT(Wl1_2b,Wh1_2b),JF(2),o1,0,0,0); OPQ(o1); s16x4 Wl0_5b=TR(wa[0][2]+10240); s16x4 Wh0_5b=TR(wa[0][2]+10752); SB;
      o0=__builtin_amdgcn_mfma_f32_32x32x16_bf16(CAT(Wl0_3b,Wh0_3b),JF(3),o0,0,0,0); OPQ(o0); s16x4 Wl1_5b=TR(wa[1][2]+10240); s16x4 Wh1_5b=TR(wa[1][2]+10752); SB;
      o1=__builtin_amdgcn_mfma_f32_32x32x16_bf16(CAT(Wl1_3b,Wh1_3b),JF(3),o1,0,0,0); OPQ(o1); s16x4 Wl0_6b=TR(wa[0][2]+12288); s16x4 Wh0_6b=TR(wa[0][2]+12800); SB;
      o0=__builtin_amdgcn_mfma_f32_32x32x16_bf16(CAT(Wl0_4b,Wh0_4b),JF(4),o0,0,0,0); OPQ(o0); s16x4 Wl1_6b=TR(wa[1][2]+12288); s16x4 Wh1_6b=TR(wa[1][2]+12800); SB;
      o1=__builtin_amdgcn_mfma_f32_32x32x16_bf16(CAT(Wl1_4b,Wh1_4b),JF(4),o1,0,0,0); OPQ(o1); s16x4 Wl0_7b=TR(wa[0][2]+14336); s16x4 Wh0_7b=TR(wa[0][2]+14848); SB;
      o0=__builtin_amdgcn_mfma_f32_32x32x16_bf16(CAT(Wl0_5b,Wh0_5b),JF(5),o0,0,0,0); OPQ(o0); s16x4 Wl1_7b=TR(wa[1][2]+14336); s16x4 Wh1_7b=TR(wa[1][2]+14848); SB;
      o1=__builtin_amdgcn_mfma_f32_32x32x16_bf16(CAT(Wl1_5b,Wh1_5b),JF(5),o1,0,0,0); OPQ(o1); s16x4 Wl0_8b=TR(wa[0][0]+0); s16x4 Wh0_8b=TR(wa[0][0]+512); SB;
      o0=__builtin_amdgcn_mfma_f32_32x32x16_bf16(CAT(Wl0_6b,Wh0_6b),JF(6),o0,0,0,0); OPQ(o0); s16x4 Wl1_8b=TR(wa[1][0]+0); s16x4 Wh1_8b=TR(wa[1][0]+512); SB;
      o1=__builtin_amdgcn_mfma_f32_32x32x16_bf16(CAT(Wl1_6b,Wh1_6b),JF(6),o1,0,0,0); OPQ(o1); s16x4 Wl0_9b=TR(wa[0][0]+2048); s16x4 Wh0_9b=TR(wa[0][0]+2560); SB;
      o0=__builtin_amdgcn_mfma_f32_32x32x16_bf16(CAT(Wl0_7b,Wh0_7b),JF(7),o0,0,0,0); OPQ(o0); s16x4 Wl1_9b=TR(wa[1][0]+2048); s16x4 Wh1_9b=TR(wa[1][0]+2560); SB;
      o1=__builtin_amdgcn_mfma_f32_32x32x16_bf16(CAT(Wl1_7b,Wh1_7b),JF(7),o1,0,0,0); OPQ(o1); s16x4 Wl0_10b=TR(wa[0][0]+4096); s16x4 Wh0_10b=TR(wa[0][0]+4608); SB;
      o0=__builtin_amdgcn_mfma_f32_32x32x16_bf16(CAT(Wl0_8b,Wh0_8b),JF(8),o0,0,0,0); OPQ(o0); s16x4 Wl1_10b=TR(wa[1][0]+4096); s16x4 Wh1_10b=TR(wa[1][0]+4608); SB;
      o1=__builtin_amdgcn_mfma_f32_32x32x16_bf16(CAT(Wl1_8b,Wh1_8b),JF(8),o1,0,0,0); OPQ(o1); s16x4 Wl0_11b=TR(wa[0][0]+6144); s16x4 Wh0_11b=TR(wa[0][0]+6656); SB;
      o0=__builtin_amdgcn_mfma_f32_32x32x16_bf16(CAT(Wl0_9b,Wh0_9b),JF(9),o0,0,0,0); OPQ(o0); s16x4 Wl1_11b=TR(wa[1][0]+6144); s16x4 Wh1_11b=TR(wa[1][0]+6656); SB;
      o1=__builtin_amdgcn_mfma_f32_32x32x16_bf16(CAT(Wl1_9b,Wh1_9b),JF(9),o1,0,0,0); OPQ(o1); s16x4 Wl0_12b=TR(wa[0][0]+8192); s16x4 Wh0_12b=TR(wa[0][0]+8704); SB;
      o0=__builtin_amdgcn_mfma_f32_32x32x16_bf16(CAT(Wl0_10b,Wh0_10b),JF(10),o0,0,0,0); OPQ(o0); s16x4 Wl1_12b=TR(wa[1][0]+8192); s16x4 Wh1_12b=TR(wa[1][0]+8704); SB;
      o1=__builtin_amdgcn_mfma_f32_32x32x16_bf16(CAT(Wl1_10b,Wh1_10b),JF(10),o1,0,0,0); OPQ(o1); s16x4 Wl0_13b=TR(wa[0][0]+10240); s16x4 Wh0_13b=TR(wa[0][0]+10752); SB;
      o0=__builtin_amdgcn_mfma_f32_32x32x16_bf16(CAT(Wl0_11b,Wh0_11b),JF(11),o0,0,0,0); OPQ(o0); s16x4 Wl1_13b=TR(wa[1][0]+10240); s16x4 Wh1_13b=TR(wa[1][0]+10752); SB;
      o1=__builtin_amdgcn_mfma_f32_32x32x16_bf16(CAT(Wl1_11b,Wh1_11b),JF(11),o1,0,0,0); OPQ(o1); s16x4 Wl0_14b=TR(wa[0][0]+12288); s16x4 Wh0_14b=TR(wa[0][0]+12800); SB;
      o0=__builtin_amdgcn_mfma_f32_32x32x16_bf16(CAT(Wl0_12b,Wh0_12b),JF(12),o0,0,0,0); OPQ(o0); s16x4 Wl1_14b=TR(wa[1][0]+12288); s16x4 Wh1_14b=TR(wa[1][0]+12800); SB;
      o1=__builtin_amdgcn_mfma_f32_32x32x16_bf16(CAT(Wl1_12b,Wh1_12b),JF(12),o1,0,0,0); OPQ(o1); s16x4 Wl0_15b=TR(wa[0][0]+14336); s16x4 Wh0_15b=TR(wa[0][0]+14848); SB;
      o0=__builtin_amdgcn_mfma_f32_32x32x16_bf16(CAT(Wl0_13b,Wh0_13b),JF(13),o0,0,0,0); OPQ(o0); s16x4 Wl1_15b=TR(wa[1][0]+14336); s16x4 Wh1_15b=TR(wa[1][0]+14848); SB;
      o1=__builtin_amdgcn_mfma_f32_32x32x16_bf16(CAT(Wl1_13b,Wh1_13b),JF(13),o1,0,0,0); OPQ(o1); Wl2_0=TR(wa[0][2]+16384); Wh2_0=TR(wa[0][2]+16896); SB;
      o0=__builtin_amdgcn_mfma_f32_32x32x16_bf16(CAT(Wl0_14b,Wh0_14b),JF(14),o0,0,0,0); OPQ(o0); Wl2_1=TR(wa[0][2]+18432); Wh2_1=TR(wa[0][2]+18944); SB;
      o1=__builtin_amdgcn_mfma_f32_32x32x16_bf16(CAT(Wl1_14b,Wh1_14b),JF(14),o1,0,0,0); OPQ(o1); Wl2_2=TR(wa[0][2]+20480); Wh2_2=TR(wa[0][2]+20992); SB;
      o0=__builtin_amdgcn_mfma_f32_32x32x16_bf16(CAT(Wl0_15b,Wh0_15b),JF(15),o0,0,0,0); OPQ(o0); Wl2_3=TR(wa[0][2]+22528); Wh2_3=TR(wa[0][2]+23040); SB;
      o1=__builtin_amdgcn_mfma_f32_32x32x16_bf16(CAT(Wl1_15b,Wh1_15b),JF(15),o1,0,0,0); OPQ(o1); Wl2_4=TR(wa[0][2]+24576); Wh2_4=TR(wa[0][2]+25088); SB;
    } else {
      if(wid<4){
        dma16(r_wo, voffWo, wid*16384 + 256, wid*4096 + 81920);
        dma16(r_wo, voffWo, wid*16384 + 4352, wid*4096 + 82944);
        dma16(r_wo, voffWo, wid*16384 + 8448, wid*4096 + 83968);
        dma16(r_wo, voffWo, wid*16384 + 12544, wid*4096 + 84992);
        dma16(r_wo, voffWo, wid*16384 + 65792, wid*4096 + 98304);
        dma16(r_wo, voffWo, wid*16384 + 69888, wid*4096 + 99328);
        dma16(r_wo, voffWo, wid*16384 + 73984, wid*4096 + 100352);
        dma16(r_wo, voffWo, wid*16384 + 78080, wid*4096 + 101376);
        dma16(r_wo, voffWo, wid*16384 + 384, wid*4096 + 114688);
        dma16(r_wo, voffWo, wid*16384 + 4480, wid*4096 + 115712);
        dma16(r_wo, voffWo, wid*16384 + 8576, wid*4096 + 116736);
        dma16(r_wo, voffWo, wid*16384 + 12672, wid*4096 + 117760);
        int rw=32768+wid*8192+(lane>>5)*512+(lane&31)*16; _Pragma("unroll") for(int i=0;i<4;i++) _Pragma("unroll") for(int c=0;c<2;c++) *(lds_f32x4_p)(lds+rw+(2*i+c)*1024)=acc[i][c]; }
      BAR();
      if(wid<4){ dma16(r_wo, voffWo, wid*16384 + 65920, wid*4096 + 65536); dma16(r_wo, voffWo, wid*16384 + 70016, wid*4096 + 66560); dma16(r_wo, voffWo, wid*16384 + 74112, wid*4096 + 67584); dma16(r_wo, voffWo, wid*16384 + 78208, wid*4096 + 68608); }
      f32x4 R[32];
      _Pragma("unroll") for(int n=0;n<16;n++){ R[2*n]=*(lds_f32x4_p)(lds+rr+n*2048); R[2*n+1]=*(lds_f32x4_p)(lds+rr+n*2048+512); }
      _Pragma("unroll") for(int n=0;n<8;n++){ slow_fix<0>(eg[n],R[4*n]); slow_fix<4>(eg[n],R[4*n+1]); slow_fix<8>(eg[n],R[4*n+2]); slow_fix<12>(eg[n],R[4*n+3]);
        OPQ(R[4*n]); OPQ(R[4*n+1]); OPQ(R[4*n+2]); OPQ(R[4*n+3]); eg[n]=1.0f; OPQ(eg[n]); SB; }
      WAITVM(0); BAR();
      s16x4 Wl0_0c=TR(wa[0][2]+0); s16x4 Wh0_0c=TR(wa[0][2]+512); s16x4 Wl1_0c=TR(wa[1][2]+0); s16x4 Wh1_0c=TR(wa[1][2]+512);
      SB; asm volatile("s_nop 1"); SB; Ja[0]=jhalf<0>(eg[0],R[0]); SB; Jb[0]=jhalf<4>(eg[0],R[1]); SB;
      o0=__builtin_amdgcn_mfma_f32_32x32x16_bf16(CAT(Wl0_0c,Wh0_0c),JF(0),z16,0,0,0); OPQ(o0); s16x4 Wl0_1c=TR(wa[0][2]+2048); s16x4 Wh0_1c=TR(wa[0][2]+2560); SB; Ja[1]=jhalf<8>(eg[0],R[2]); SB;
      o1=__builtin_amdgcn_mfma_f32_32x32x16_bf16(CAT(Wl1_0c,Wh1_0c),JF(0),z16,0,0,0); OPQ(o1); s16x4 Wl1_1c=TR(wa[1][2]+2048); s16x4 Wh1_1c=TR(wa[1][2]+2560); SB; Jb[1]=jhalf<12>(eg[0],R[3]); SB;
      o0=__builtin_amdgcn_mfma_f32_32x32x16_bf16(CAT(Wl0_1c,Wh0_1c),JF(1),o0,0,0,0); OPQ(o0); s16x4 Wl0_2c=TR(wa[0][2]+4096); s16x4 Wh0_2c=TR(wa[0][2]+4608); SB; Ja[2]=jhalf<0>(eg[1],R[4]); SB;
      o1=__builtin_amdgcn_mfma_f32_32x32x16_bf16(CAT(Wl1_1c,Wh1_1c),JF(1),o1,0,0,0); OPQ(o1); s16x4 Wl1_2c=TR(wa[1][2]+4096); s16x4 Wh1_2c=TR(wa[1][2]+4608); SB; Jb[2]=jhalf<4>(eg[1],R[5]); SB;
      o0=__builtin_amdgcn_mfma_f32_32x32x16_bf16(CAT(Wl0_2c,Wh0_2c),JF(2),o0,0,0,0); OPQ(o0); s16x4 Wl0_3c=TR(wa[0][2]+6144); s16x4 Wh0_3c=TR(wa[0][2]+6656); SB; Ja[3]=jhalf<8>(eg[1],R[6]); SB;
      o1=__builtin_amdgcn_mfma_f32_32x32x16_bf16(CAT(Wl1_2c,Wh1_2c),JF(2),o1,0,0,0); OPQ(o1); s16x4 Wl1_3c=TR(wa[1][2]+6144); s16x4 Wh1_3c=TR(wa[1][2]+6656); SB; Jb[3]=jhalf<12>(eg[1],R[7]); SB;
      o0=__builtin_amdgcn_mfma_f32_32x32x16_bf16(CAT(Wl0_3c,Wh0_3c),JF(3),o0,0,0,0); OPQ(o0); s16x4 Wl0_4c=TR(wa[0][2]+8192); s16x4 Wh0_4c=TR(wa[0][2]+8704); SB; Ja[4]=jhalf<0>(eg[2],R[8]); SB;
      o1=__builtin_amdgcn_mfma_f32_32x32x16_bf16(CAT(Wl1_3c,Wh1_3c),JF(3),o1,0,0,0); OPQ(o1); s16x4 Wl1_4c=TR(wa[1][2]+8192); s16x4 Wh1_4c=TR(wa[1][2]+8704); SB; Jb[4]=jhalf<4>(eg[2],R[9]); SB;
      o0=__builtin_amdgcn_mfma_f32_32x32x16_bf16(CAT(Wl0_4c,Wh0_4c),JF(4),o0,0,0,0); OPQ(o0); s16x4 Wl0_5c=TR(wa[0][2]+10240); s16x4 Wh0_5c=TR(wa[0][2]+10752); SB; Ja[5]=jhalf<8>(eg[2],R[10]); SB;
      o1=__builtin_amdgcn_mfma_f32_32x32x16_bf16(CAT(Wl1_4c,Wh1_4c),JF(4),o1,0,0,0); OPQ(o1); s16x4 Wl1_5c=TR(wa[1][2]+10240); s16x4 Wh1_5c=TR(wa[1][2]+10752); SB; Jb[5]=jhalf<12>(eg[2],R[11]); SB;
      o0=__builtin_amdgcn_mfma_f32_32x32x16_bf16(CAT(Wl0_5c,Wh0_5c),JF(5),o0,0,0,0); OPQ(o0); s16x4 Wl0_6c=TR(wa[0][2]+12288); s16x4 Wh0_6c=TR(wa[0][2]+12800); SB; Ja[6]=jhalf<0>(eg[3],R[12]); SB;
      o1=__builtin_amdgcn_mfma_f32_32x32x16_bf16(CAT(Wl1_5c,Wh1_5c),JF(5),o1,0,0,0); OPQ(o1); s16x4 Wl1_6c=TR(wa[1][2]+12288); s16x4 Wh1_6c=TR(wa[1][2]+12800); SB; Jb[6]=jhalf<4>(eg[3],R[13]); SB;
      o0=__builtin_amdgcn_mfma_f32_32x32x16_bf16(CAT(Wl0_6c,Wh0_6c),JF(6),o0,0,0,0); OPQ(o0); s16x4 Wl0_7c=TR(wa[0][2]+14336); s16x4 Wh0_7c=TR(wa[0][2]+14848); SB; Ja[7]=jhalf<8>(eg[3],R[14]); SB;
      o1=__builtin_amdgcn_mfma_f32_32x32x16_bf16(CAT(Wl1_6c,Wh1_6c),JF(6),o1,0,0,0); OPQ(o1); s16x4 Wl1_7c=TR(wa[1][2]+14336); s16x4 Wh1_7c=TR(wa[1][2]+14848); SB; Jb[7]=jhalf<12>(eg[3],R[15]); SB;
      o0=__builtin_amdgcn_mfma_f32_32x32x16_bf16(CAT(Wl0_7c,Wh0_7c),JF(7),o0,0,0,0); OPQ(o0); s16x4 Wl0_8c=TR(wa[0][0]+0); s16x4 Wh0_8c=TR(wa[0][0]+512); SB; Ja[8]=jhalf<0>(eg[4],R[16]); SB;
      o1=__builtin_amdgcn_mfma_f32_32x32x16_bf16(CAT(Wl1_7c,Wh1_7c),JF(7),o1,0,0,0); OPQ(o1); s16x4 Wl1_8c=TR(wa[1][0]+0); s16x4 Wh1_8c=TR(wa[1][0]+512); SB; Jb[8]=jhalf<4>(eg[4],R[17]); SB;
      o0=__builtin_amdgcn_mfma_f32_32x32x16_bf16(CAT(Wl0_8c,Wh0_8c),JF(8),o0,0,0,0); OPQ(o0); s16x4 Wl0_9c=TR(wa[0][0]+2048); s16x4 Wh0_9c=TR(wa[0][0]+2560); SB; Ja[9]=jhalf<8>(eg[4],R[18]); SB;
      o1=__builtin_amdgcn_mfma_f32_32x32x16_bf16(CAT(Wl1_8c,Wh1_8c),JF(8),o1,0,0,0); OPQ(o1); s16x4 Wl1_9c=TR(wa[1][0]+2048); s16x4 Wh1_9c=TR(wa[1][0]+2560); SB; Jb[9]=jhalf<12>(eg[4],R[19]); SB;
      o0=__builtin_amdgcn_mfma_f32_32x32x16_bf16(CAT(Wl0_9c,Wh0_9c),JF(9),o0,0,0,0); OPQ(o0); s16x4 Wl0_10c=TR(wa[0][0]+4096); s16x4 Wh0_10c=TR(wa[0][0]+4608); SB; Ja[10]=jhalf<0>(eg[5],R[20]); SB;
      o1=__builtin_amdgcn_mfma_f32_32x32x16_bf16(CAT(Wl1_9c,Wh1_9c),JF(9),o1,0,0,0); OPQ(o1); s16x4 Wl1_10c=TR(wa[1][0]+4096); s16x4 Wh1_10c=TR(wa[1][0]+4608); SB; Jb[10]=jhalf<4>(eg[5],R[21]); SB;
      o0=__builtin_amdgcn_mfma_f32_32x32x16_bf16(CAT(Wl0_10c,Wh0_10c),JF(10),o0,0,0,0); OPQ(o0); s16x4 Wl0_11c=TR(wa[0][0]+6144); s16x4 Wh0_11c=TR(wa[0][0]+6656); SB; Ja[11]=jhalf<8>(eg[5],R[22]); SB;
      o1=__builtin_amdgcn_mfma_f32_32x32x16_bf16(CAT(Wl1_10c,Wh1_10c),JF(10),o1,0,0,0); OPQ(o1); s16x4 Wl1_11c=TR(wa[1][0]+6144); s16x4 Wh1_11c=TR(wa[1][0]+6656); SB; Jb[11]=jhalf<12>(eg[5],R[23]); SB;
      o0=__builtin_amdgcn_mfma_f32_32x32x16_bf16(CAT(Wl0_11c,Wh0_11c),JF(11),o0,0,0,0); OPQ(o0); s16x4 Wl0_12c=TR(wa[0][0]+8192); s16x4 Wh0_12c=TR(wa[0][0]+8704); SB; Ja[12]=jhalf<0>(eg[6],R[24]); SB;
      o1=__builtin_amdgcn_mfma_f32_32x32x16_bf16(CAT(Wl1_11c,Wh1_11c),JF(11),o1,0,0,0); OPQ(o1); s16x4 Wl1_12c=TR(wa[1][0]+8192); s16x4 Wh1_12c=TR(wa[1][0]+8704); SB; Jb[12]=jhalf<4>(eg[6],R[25]); SB;
      o0=__builtin_amdgcn_mfma_f32_32x32x16_bf16(CAT(Wl0_12c,Wh0_12c),JF(12),o0,0,0,0); OPQ(o0); s16x4 Wl0_13c=TR(wa[0][0]+10240); s16x4 Wh0_13c=TR(wa[0][0]+10752); SB; Ja[13]=jhalf<8>(eg[6],R[26]); SB;
      o1=__builtin_amdgcn_mfma_f32_32x32x16_bf16(CAT(Wl1_12c,Wh1_12c),JF(12),o1,0,0,0); OPQ(o1); s16x4 Wl1_13c=TR(wa[1][0]+10240); s16x4 Wh1_13c=TR(wa[1][0]+10752); SB; Jb[13]=jhalf<12>(eg[6],R[27]); SB;
      o0=__builtin_amdgcn_mfma_f32_32x32x16_bf16(CAT(Wl0_13c,Wh0_13c),JF(13),o0,0,0,0); OPQ(o0); s16x4 Wl0_14c=TR(wa[0][0]+12288); s16x4 Wh0_14c=TR(wa[0][0]+12800); SB; Ja[14]=jhalf<0>(eg[7],R[28]); SB;
      o1=__builtin_amdgcn_mfma_f32_32x32x16_bf16(CAT(Wl1_13c,Wh1_13c),JF(13),o1,0,0,0); OPQ(o1); s16x4 Wl1_14c=TR(wa[1][0]+12288); s16x4 Wh1_14c=TR(wa[1][0]+12800); SB; Jb[14]=jhalf<4>(eg[7],R[29]); SB;
      o0=__builtin_amdgcn_mfma_f32_32x32x16_bf16(CAT(Wl0_14c,Wh0_14c),JF(14),o0,0,0,0); OPQ(o0); s16x4 Wl0_15c=TR(wa[0][0]+14336); s16x4 Wh0_15c=TR(wa[0][0]+14848); SB; Ja[15]=jhalf<8>(eg[7],R[30]); SB;
      o1=__builtin_amdgcn_mfma_f32_32x32x16_bf16(CAT(Wl1_14c,Wh1_14c),JF(14),o1,0,0,0); OPQ(o1); s16x4 Wl1_15c=TR(wa[1][0]+14336); s16x4 Wh1_15c=TR(wa[1][0]+14848); SB; Jb[15]=jhalf<12>(eg[7],R[31]); SB;
      o0=__builtin_amdgcn_mfma_f32_32x32x16_bf16(CAT(Wl0_15c,Wh0_15c),JF(15),o0,0,0,0); OPQ(o0); Wl2_0=TR(wa[0][2]+16384); Wh2_0=TR(wa[0][2]+16896); Wl2_2=TR(wa[0][2]+20480); Wh2_2=TR(wa[0][2]+20992); Wl2_4=TR(wa[0][2]+24576); Wh2_4=TR(wa[0][2]+25088); SB;  SB;
      o1=__builtin_amdgcn_mfma_f32_32x32x16_bf16(CAT(Wl1_15c,Wh1_15c),JF(15),o1,0,0,0); OPQ(o1); Wl2_1=TR(wa[0][2]+18432); Wh2_1=TR(wa[0][2]+18944); Wl2_3=TR(wa[0][2]+22528); Wh2_3=TR(wa[0][2]+23040); SB;  SB;
    }
        f32x16 o2=__builtin_amdgcn_mfma_f32_32x32x16_bf16(CAT(Wl2_0,Wh2_0),JF(0),z16,0,0,0); OPQ(o2); s16x4 Wl2_5=TR(wa[0][2]+26624); s16x4 Wh2_5=TR(wa[0][2]+27136); SB;  SB;
    o2=__builtin_amdgcn_mfma_f32_32x32x16_bf16(CAT(Wl2_1,Wh2_1),JF(1),o2,0,0,0); OPQ(o2); s16x4 Wl2_6=TR(wa[0][2]+28672); s16x4 Wh2_6=TR(wa[0][2]+29184); SB; unsigned pk0_0=pk2x2(o0[0],o0[1]); OPQ(pk0_0); unsigned pk0_1=pk2x2(o0[2],o0[3]); OPQ(pk0_1); unsigned pk0_2=pk2x2(o0[4],o0[5]); OPQ(pk0_2); SB;
    o2=__builtin_amdgcn_mfma_f32_32x32x16_bf16(CAT(Wl2_2,Wh2_2),JF(2),o2,0,0,0); OPQ(o2); s16x4 Wl2_7=TR(wa[0][2]+30720); s16x4 Wh2_7=TR(wa[0][2]+31232); SB; unsigned pk0_3=pk2x2(o0[6],o0[7]); OPQ(pk0_3); unsigned pk0_4=pk2x2(o0[8],o0[9]); OPQ(pk0_4); SB;
    o2=__builtin_amdgcn_mfma_f32_32x32x16_bf16(CAT(Wl2_3,Wh2_3),JF(3),o2,0,0,0); OPQ(o2); s16x4 Wl2_8=TR(wa[0][0]+16384); s16x4 Wh2_8=TR(wa[0][0]+16896); SB; unsigned pk0_5=pk2x2(o0[10],o0[11]); OPQ(pk0_5); unsigned pk0_6=pk2x2(o0[12],o0[13]); OPQ(pk0_6); unsigned pk0_7=pk2x2(o0[14],o0[15]); OPQ(pk0_7); SB;
    o2=__builtin_amdgcn_mfma_f32_32x32x16_bf16(CAT(Wl2_4,Wh2_4),JF(4),o2,0,0,0); OPQ(o2); s16x4 Wl2_9=TR(wa[0][0]+18432); s16x4 Wh2_9=TR(wa[0][0]+18944); SB; unsigned pk1_0=pk2x2(o1[0],o1[1]); OPQ(pk1_0); unsigned pk1_1=pk2x2(o1[2],o1[3]); OPQ(pk1_1); SB;
    o2=__builtin_amdgcn_mfma_f32_32x32x16_bf16(CAT(Wl2_5,Wh2_5),JF(5),o2,0,0,0); OPQ(o2); s16x4 Wl2_10=TR(wa[0][0]+20480); s16x4 Wh2_10=TR(wa[0][0]+20992); SB; unsigned pk1_2=pk2x2(o1[4],o1[5]); OPQ(pk1_2); unsigned pk1_3=pk2x2(o1[6],o1[7]); OPQ(pk1_3); unsigned pk1_4=pk2x2(o1[8],o1[9]); OPQ(pk1_4); SB;
    o2=__builtin_amdgcn_mfma_f32_32x32x16_bf16(CAT(Wl2_6,Wh2_6),JF(6),o2,0,0,0); OPQ(o2); s16x4 Wl2_11=TR(wa[0][0]+22528); s16x4 Wh2_11=TR(wa[0][0]+23040); SB; unsigned pk1_5=pk2x2(o1[10],o1[11]); OPQ(pk1_5); unsigned pk1_6=pk2x2(o1[12],o1[13]); OPQ(pk1_6); SB;
    o2=__builtin_amdgcn_mfma_f32_32x32x16_bf16(CAT(Wl2_7,Wh2_7),JF(7),o2,0,0,0); OPQ(o2); s16x4 Wl2_12=TR(wa[0][0]+24576); s16x4 Wh2_12=TR(wa[0][0]+25088); SB; unsigned pk1_7=pk2x2(o1[14],o1[15]); OPQ(pk1_7); u32x4 Y0_0,Y0_1,Y0_2,Y0_3,Z0_0,Z0_1,Z0_2,Z0_3; SB;
    o2=__builtin_amdgcn_mfma_f32_32x32x16_bf16(CAT(Wl2_8,Wh2_8),JF(8),o2,0,0,0); OPQ(o2); s16x4 Wl2_13=TR(wa[0][0]+26624); s16x4 Wh2_13=TR(wa[0][0]+27136); SB;  SB;
    o2=__builtin_amdgcn_mfma_f32_32x32x16_bf16(CAT(Wl2_9,Wh2_9),JF(9),o2,0,0,0); OPQ(o2); s16x4 Wl2_14=TR(wa[0][0]+28672); s16x4 Wh2_14=TR(wa[0][0]+29184); SB; Y0_0=qsel<0x55555555u,0xB1>((u32x4){pk0_4,pk0_5,pk0_6,pk0_7},(u32x4){pk0_0,pk0_1,pk0_2,pk0_3}); SB;
    o2=__builtin_amdgcn_mfma_f32_32x32x16_bf16(CAT(Wl2_10,Wh2_10),JF(10),o2,0,0,0); OPQ(o2); s16x4 Wl2_15=TR(wa[0][0]+30720); s16x4 Wh2_15=TR(wa[0][0]+31232); SB; Y0_1=qsel<0xAAAAAAAAu,0xB1>((u32x4){pk0_0,pk0_1,pk0_2,pk0_3},(u32x4){pk0_4,pk0_5,pk0_6,pk0_7}); SB;
    o2=__builtin_amdgcn_mfma_f32_32x32x16_bf16(CAT(Wl2_11,Wh2_11),JF(11),o2,0,0,0); OPQ(o2); s16x4 Wl3_0=TR(wa[1][2]+16384); s16x4 Wh3_0=TR(wa[1][2]+16896); SB;  SB;
    o2=__builtin_amdgcn_mfma_f32_32x32x16_bf16(CAT(Wl2_12,Wh2_12),JF(12),o2,0,0,0); OPQ(o2); s16x4 Wl3_1=TR(wa[1][2]+18432); s16x4 Wh3_1=TR(wa[1][2]+18944); SB; Y0_2=qsel<0x55555555u,0xB1>((u32x4){pk1_4,pk1_5,pk1_6,pk1_7},(u32x4){pk1_0,pk1_1,pk1_2,pk1_3}); SB;
    o2=__builtin_amdgcn_mfma_f32_32x32x16_bf16(CAT(Wl2_13,Wh2_13),JF(13),o2,0,0,0); OPQ(o2); s16x4 Wl3_2=TR(wa[1][2]+20480); s16x4 Wh3_2=TR(wa[1][2]+20992); SB; Y0_3=qsel<0xAAAAAAAAu,0xB1>((u32x4){pk1_0,pk1_1,pk1_2,pk1_3},(u32x4){pk1_4,pk1_5,pk1_6,pk1_7}); SB;
    o2=__builtin_amdgcn_mfma_f32_32x32x16_bf16(CAT(Wl2_14,Wh2_14),JF(14),o2,0,0,0); OPQ(o2); s16x4 Wl3_3=TR(wa[1][2]+22528); s16x4 Wh3_3=TR(wa[1][2]+23040); SB;  SB;
    o2=__builtin_amdgcn_mfma_f32_32x32x16_bf16(CAT(Wl2_15,Wh2_15),JF(15),o2,0,0,0); OPQ(o2); s16x4 Wl3_4=TR(wa[1][2]+24576); s16x4 Wh3_4=TR(wa[1][2]+25088); SB; Z0_0=qsel<0x33333333u,0x4E>(Y0_2,Y0_0); OSTORE(Z0_0,vo+0,0); SB;
    f32x16 o3=__builtin_amdgcn_mfma_f32_32x32x16_bf16(CAT(Wl3_0,Wh3_0),JF(0),z16,0,0,0); OPQ(o3); s16x4 Wl3_5=TR(wa[1][2]+26624); s16x4 Wh3_5=TR(wa[1][2]+27136); SB;  SB;
    o3=__builtin_amdgcn_mfma_f32_32x32x16_bf16(CAT(Wl3_1,Wh3_1),JF(1),o3,0,0,0); OPQ(o3); s16x4 Wl3_6=TR(wa[1][2]+28672); s16x4 Wh3_6=TR(wa[1][2]+29184); SB; Z0_1=qsel<0x33333333u,0x4E>(Y0_3,Y0_1); OSTORE(Z0_1,vo+0,4096); SB;
    o3=__builtin_amdgcn_mfma_f32_32x32x16_bf16(CAT(Wl3_2,Wh3_2),JF(2),o3,0,0,0); OPQ(o3); s16x4 Wl3_7=TR(wa[1][2]+30720); s16x4 Wh3_7=TR(wa[1][2]+31232); SB;  SB;
    o3=__builtin_amdgcn_mfma_f32_32x32x16_bf16(CAT(Wl3_3,Wh3_3),JF(3),o3,0,0,0); OPQ(o3); s16x4 Wl3_8=TR(wa[1][0]+16384); s16x4 Wh3_8=TR(wa[1][0]+16896); SB; Z0_2=qsel<0xCCCCCCCCu,0x4E>(Y0_0,Y0_2); OSTORE(Z0_2,vo+0,8192); SB;
    o3=__builtin_amdgcn_mfma_f32_32x32x16_bf16(CAT(Wl3_4,Wh3_4),JF(4),o3,0,0,0); OPQ(o3); s16x4 Wl3_9=TR(wa[1][0]+18432); s16x4 Wh3_9=TR(wa[1][0]+18944); SB;  SB;
    o3=__builtin_amdgcn_mfma_f32_32x32x16_bf16(CAT(Wl3_5,Wh3_5),JF(5),o3,0,0,0); OPQ(o3); s16x4 Wl3_10=TR(wa[1][0]+20480); s16x4 Wh3_10=TR(wa[1][0]+20992); SB; Z0_3=qsel<0xCCCCCCCCu,0x4E>(Y0_1,Y0_3); OSTORE(Z0_3,vo+0,12288); SB;
    o3=__builtin_amdgcn_mfma_f32_32x32x16_bf16(CAT(Wl3_6,Wh3_6),JF(6),o3,0,0,0); OPQ(o3); s16x4 Wl3_11=TR(wa[1][0]+22528); s16x4 Wh3_11=TR(wa[1][0]+23040); SB;  SB;
    o3=__builtin_amdgcn_mfma_f32_32x32x16_bf16(CAT(Wl3_7,Wh3_7),JF(7),o3,0,0,0); OPQ(o3); s16x4 Wl3_12=TR(wa[1][0]+24576); s16x4 Wh3_12=TR(wa[1][0]+25088); SB; unsigned pk2_0=pk2x2(o2[0],o2[1]); OPQ(pk2_0); unsigned pk2_1=pk2x2(o2[2],o2[3]); OPQ(pk2_1); SB;
    o3=__builtin_amdgcn_mfma_f32_32x32x16_bf16(CAT(Wl3_8,Wh3_8),JF(8),o3,0,0,0); OPQ(o3); s16x4 Wl3_13=TR(wa[1][0]+26624); s16x4 Wh3_13=TR(wa[1][0]+27136); SB; unsigned pk2_2=pk2x2(o2[4],o2[5]); OPQ(pk2_2); unsigned pk2_3=pk2x2(o2[6],o2[7]); OPQ(pk2_3); SB;
    o3=__builtin_amdgcn_mfma_f32_32x32x16_bf16(CAT(Wl3_9,Wh3_9),JF(9),o3,0,0,0); OPQ(o3); s16x4 Wl3_14=TR(wa[1][0]+28672); s16x4 Wh3_14=TR(wa[1][0]+29184); SB; unsigned pk2_4=pk2x2(o2[8],o2[9]); OPQ(pk2_4); unsigned pk2_5=pk2x2(o2[10],o2[11]); OPQ(pk2_5); unsigned pk2_6=pk2x2(o2[12],o2[13]); OPQ(pk2_6); SB;
    o3=__builtin_amdgcn_mfma_f32_32x32x16_bf16(CAT(Wl3_10,Wh3_10),JF(10),o3,0,0,0); OPQ(o3); s16x4 Wl3_15=TR(wa[1][0]+30720); s16x4 Wh3_15=TR(wa[1][0]+31232); SB; unsigned pk2_7=pk2x2(o2[14],o2[15]); OPQ(pk2_7); SB;
    o3=__builtin_amdgcn_mfma_f32_32x32x16_bf16(CAT(Wl3_11,Wh3_11),JF(11),o3,0,0,0); OPQ(o3); s16x4 Wl4_0=TR(wa[0][1]+16384); s16x4 Wh4_0=TR(wa[0][1]+16896); SB;  SB;
    o3=__builtin_amdgcn_mfma_f32_32x32x16_bf16(CAT(Wl3_12,Wh3_12),JF(12),o3,0,0,0); OPQ(o3); s16x4 Wl4_1=TR(wa[0][1]+18432); s16x4 Wh4_1=TR(wa[0][1]+18944); SB;  SB;
    o3=__builtin_amdgcn_mfma_f32_32x32x16_bf16(CAT(Wl3_13,Wh3_13),JF(13),o3,0,0,0); OPQ(o3); s16x4 Wl4_2=TR(wa[0][1]+20480); s16x4 Wh4_2=TR(wa[0][1]+20992); SB;  SB;
    o3=__builtin_amdgcn_mfma_f32_32x32x16_bf16(CAT(Wl3_14,Wh3_14),JF(14),o3,0,0,0); OPQ(o3); s16x4 Wl4_3=TR(wa[0][1]+22528); s16x4 Wh4_3=TR(wa[0][1]+23040); SB;  SB;
    o3=__builtin_amdgcn_mfma_f32_32x32x16_bf16(CAT(Wl3_15,Wh3_15),JF(15),o3,0,0,0); OPQ(o3); s16x4 Wl4_4=TR(wa[0][1]+24576); s16x4 Wh4_4=TR(wa[0][1]+25088); SB;  SB;
        f32x16 o4=__builtin_amdgcn_mfma_f32_32x32x16_bf16(CAT(Wl4_0,Wh4_0),JF(0),z16,0,0,0); OPQ(o4); s16x4 Wl4_5=TR(wa[0][1]+26624); s16x4 Wh4_5=TR(wa[0][1]+27136); SB;  SB;
    o4=__builtin_amdgcn_mfma_f32_32x32x16_bf16(CAT(Wl4_1,Wh4_1),JF(1),o4,0,0,0); OPQ(o4); s16x4 Wl4_6=TR(wa[0][1]+28672); s16x4 Wh4_6=TR(wa[0][1]+29184); SB; unsigned pk3_0=pk2x2(o3[0],o3[1]); OPQ(pk3_0); unsigned pk3_1=pk2x2(o3[2],o3[3]); OPQ(pk3_1); SB;
    o4=__builtin_amdgcn_mfma_f32_32x32x16_bf16(CAT(Wl4_2,Wh4_2),JF(2),o4,0,0,0); OPQ(o4); s16x4 Wl4_7=TR(wa[0][1]+30720); s16x4 Wh4_7=TR(wa[0][1]+31232); SB; unsigned pk3_2=pk2x2(o3[4],o3[5]); OPQ(pk3_2); unsigned pk3_3=pk2x2(o3[6],o3[7]); OPQ(pk3_3); SB;
    o4=__builtin_amdgcn_mfma_f32_32x32x16_bf16(CAT(Wl4_3,Wh4_3),JF(3),o4,0,0,0); OPQ(o4); s16x4 Wl4_8=TR(wa[0][1]+32768); s16x4 Wh4_8=TR(wa[0][1]+33280); SB; unsigned pk3_4=pk2x2(o3[8],o3[9]); OPQ(pk3_4); unsigned pk3_5=pk2x2(o3[10],o3[11]); OPQ(pk3_5); unsigned pk3_6=pk2x2(o3[12],o3[13]); OPQ(pk3_6); SB;
    o4=__builtin_amdgcn_mfma_f32_32x32x16_bf16(CAT(Wl4_4,Wh4_4),JF(4),o4,0,0,0); OPQ(o4); s16x4 Wl4_9=TR(wa[0][1]+34816); s16x4 Wh4_9=TR(wa[0][1]+35328); SB; unsigned pk3_7=pk2x2(o3[14],o3[15]); OPQ(pk3_7); u32x4 Y1_0,Y1_1,Y1_2,Y1_3,Z1_0,Z1_1,Z1_2,Z1_3; SB;
    o4=__builtin_amdgcn_mfma_f32_32x32x16_bf16(CAT(Wl4_5,Wh4_5),JF(5),o4,0,0,0); OPQ(o4); s16x4 Wl4_10=TR(wa[0][1]+36864); s16x4 Wh4_10=TR(wa[0][1]+37376); SB;  SB;
    o4=__builtin_amdgcn_mfma_f32_32x32x16_bf16(CAT(Wl4_6,Wh4_6),JF(6),o4,0,0,0); OPQ(o4); s16x4 Wl4_11=TR(wa[0][1]+38912); s16x4 Wh4_11=TR(wa[0][1]+39424); SB; Y1_0=qsel<0x55555555u,0xB1>((u32x4){pk2_4,pk2_5,pk2_6,pk2_7},(u32x4){pk2_0,pk2_1,pk2_2,pk2_3}); SB;
    o4=__builtin_amdgcn_mfma_f32_32x32x16_bf16(CAT(Wl4_7,Wh4_7),JF(7),o4,0,0,0); OPQ(o4); s16x4 Wl4_12=TR(wa[0][1]+40960); s16x4 Wh4_12=TR(wa[0][1]+41472); SB; Y1_1=qsel<0xAAAAAAAAu,0xB1>((u32x4){pk2_0,pk2_1,pk2_2,pk2_3},(u32x4){pk2_4,pk2_5,pk2_6,pk2_7}); SB;
    o4=__builtin_amdgcn_mfma_f32_32x32x16_bf16(CAT(Wl4_8,Wh4_8),JF(8),o4,0,0,0); OPQ(o4); s16x4 Wl4_13=TR(wa[0][1]+43008); s16x4 Wh4_13=TR(wa[0][1]+43520); SB;  SB;
    o4=__builtin_amdgcn_mfma_f32_32x32x16_bf16(CAT(Wl4_9,Wh4_9),JF(9),o4,0,0,0); OPQ(o4); s16x4 Wl4_14=TR(wa[0][1]+45056); s16x4 Wh4_14=TR(wa[0][1]+45568); SB; Y1_2=qsel<0x55555555u,0xB1>((u32x4){pk3_4,pk3_5,pk3_6,pk3_7},(u32x4){pk3_0,pk3_1,pk3_2,pk3_3}); SB;
    o4=__builtin_amdgcn_mfma_f32_32x32x16_bf16(CAT(Wl4_10,Wh4_10),JF(10),o4,0,0,0); OPQ(o4); s16x4 Wl4_15=TR(wa[0][1]+47104); s16x4 Wh4_15=TR(wa[0][1]+47616); SB;  SB;
    o4=__builtin_amdgcn_mfma_f32_32x32x16_bf16(CAT(Wl4_11,Wh4_11),JF(11),o4,0,0,0); OPQ(o4); s16x4 Wl5_0=TR(wa[1][1]+16384); s16x4 Wh5_0=TR(wa[1][1]+16896); SB; Y1_3=qsel<0xAAAAAAAAu,0xB1>((u32x4){pk3_0,pk3_1,pk3_2,pk3_3},(u32x4){pk3_4,pk3_5,pk3_6,pk3_7}); SB;
    o4=__builtin_amdgcn_mfma_f32_32x32x16_bf16(CAT(Wl4_12,Wh4_12),JF(12),o4,0,0,0); OPQ(o4); s16x4 Wl5_1=TR(wa[1][1]+18432); s16x4 Wh5_1=TR(wa[1][1]+18944); SB; Z1_0=qsel<0x33333333u,0x4E>(Y1_2,Y1_0); SB;
    o4=__builtin_amdgcn_mfma_f32_32x32x16_bf16(CAT(Wl4_13,Wh4_13),JF(13),o4,0,0,0); OPQ(o4); s16x4 Wl5_2=TR(wa[1][1]+20480); s16x4 Wh5_2=TR(wa[1][1]+20992); SB; OSTORE(Z1_0,vo+128,0); SB;
    o4=__builtin_amdgcn_mfma_f32_32x32x16_bf16(CAT(Wl4_14,Wh4_14),JF(14),o4,0,0,0); OPQ(o4); s16x4 Wl5_3=TR(wa[1][1]+22528); s16x4 Wh5_3=TR(wa[1][1]+23040); SB; Z1_1=qsel<0x33333333u,0x4E>(Y1_3,Y1_1); SB;
    o4=__builtin_amdgcn_mfma_f32_32x32x16_bf16(CAT(Wl4_15,Wh4_15),JF(15),o4,0,0,0); OPQ(o4); s16x4 Wl5_4=TR(wa[1][1]+24576); s16x4 Wh5_4=TR(wa[1][1]+25088); SB; OSTORE(Z1_1,vo+128,4096); SB;
    f32x16 o5=__builtin_amdgcn_mfma_f32_32x32x16_bf16(CAT(Wl5_0,Wh5_0),JF(0),z16,0,0,0); OPQ(o5); s16x4 Wl5_5=TR(wa[1][1]+26624); s16x4 Wh5_5=TR(wa[1][1]+27136); SB; Z1_2=qsel<0xCCCCCCCCu,0x4E>(Y1_0,Y1_2); SB;
    o5=__builtin_amdgcn_mfma_f32_32x32x16_bf16(CAT(Wl5_1,Wh5_1),JF(1),o5,0,0,0); OPQ(o5); s16x4 Wl5_6=TR(wa[1][1]+28672); s16x4 Wh5_6=TR(wa[1][1]+29184); SB; OSTORE(Z1_2,vo+128,8192); SB;
    o5=__builtin_amdgcn_mfma_f32_32x32x16_bf16(CAT(Wl5_2,Wh5_2),JF(2),o5,0,0,0); OPQ(o5); s16x4 Wl5_7=TR(wa[1][1]+30720); s16x4 Wh5_7=TR(wa[1][1]+31232); SB; Z1_3=qsel<0xCCCCCCCCu,0x4E>(Y1_1,Y1_3); SB;
    o5=__builtin_amdgcn_mfma_f32_32x32x16_bf16(CAT(Wl5_3,Wh5_3),JF(3),o5,0,0,0); OPQ(o5); s16x4 Wl5_8=TR(wa[1][1]+32768); s16x4 Wh5_8=TR(wa[1][1]+33280); SB; OSTORE(Z1_3,vo+128,12288); SB;
    o5=__builtin_amdgcn_mfma_f32_32x32x16_bf16(CAT(Wl5_4,Wh5_4),JF(4),o5,0,0,0); OPQ(o5); s16x4 Wl5_9=TR(wa[1][1]+34816); s16x4 Wh5_9=TR(wa[1][1]+35328); SB; unsigned pk4_0=pk2x2(o4[0],o4[1]); OPQ(pk4_0); SB;
    o5=__builtin_amdgcn_mfma_f32_32x32x16_bf16(CAT(Wl5_5,Wh5_5),JF(5),o5,0,0,0); OPQ(o5); s16x4 Wl5_10=TR(wa[1][1]+36864); s16x4 Wh5_10=TR(wa[1][1]+37376); SB; unsigned pk4_1=pk2x2(o4[2],o4[3]); OPQ(pk4_1); unsigned pk4_2=pk2x2(o4[4],o4[5]); OPQ(pk4_2); unsigned pk4_3=pk2x2(o4[6],o4[7]); OPQ(pk4_3); SB;
    o5=__builtin_amdgcn_mfma_f32_32x32x16_bf16(CAT(Wl5_6,Wh5_6),JF(6),o5,0,0,0); OPQ(o5); s16x4 Wl5_11=TR(wa[1][1]+38912); s16x4 Wh5_11=TR(wa[1][1]+39424); SB; unsigned pk4_4=pk2x2(o4[8],o4[9]); OPQ(pk4_4); unsigned pk4_5=pk2x2(o4[10],o4[11]); OPQ(pk4_5); SB;
    o5=__builtin_amdgcn_mfma_f32_32x32x16_bf16(CAT(Wl5_7,Wh5_7),JF(7),o5,0,0,0); OPQ(o5); s16x4 Wl5_12=TR(wa[1][1]+40960); s16x4 Wh5_12=TR(wa[1][1]+41472); SB; unsigned pk4_6=pk2x2(o4[12],o4[13]); OPQ(pk4_6); unsigned pk4_7=pk2x2(o4[14],o4[15]); OPQ(pk4_7); SB;
    o5=__builtin_amdgcn_mfma_f32_32x32x16_bf16(CAT(Wl5_8,Wh5_8),JF(8),o5,0,0,0); OPQ(o5); s16x4 Wl5_13=TR(wa[1][1]+43008); s16x4 Wh5_13=TR(wa[1][1]+43520); SB;  SB;
    o5=__builtin_amdgcn_mfma_f32_32x32x16_bf16(CAT(Wl5_9,Wh5_9),JF(9),o5,0,0,0); OPQ(o5); s16x4 Wl5_14=TR(wa[1][1]+45056); s16x4 Wh5_14=TR(wa[1][1]+45568); SB;  SB;
    o5=__builtin_amdgcn_mfma_f32_32x32x16_bf16(CAT(Wl5_10,Wh5_10),JF(10),o5,0,0,0); OPQ(o5); s16x4 Wl5_15=TR(wa[1][1]+47104); s16x4 Wh5_15=TR(wa[1][1]+47616); SB;  SB;
    o5=__builtin_amdgcn_mfma_f32_32x32x16_bf16(CAT(Wl5_11,Wh5_11),JF(11),o5,0,0,0); OPQ(o5); s16x4 Wl6_0=TR(wa[0][1]+49152); s16x4 Wh6_0=TR(wa[0][1]+49664); SB;  SB;
    o5=__builtin_amdgcn_mfma_f32_32x32x16_bf16(CAT(Wl5_12,Wh5_12),JF(12),o5,0,0,0); OPQ(o5); s16x4 Wl6_1=TR(wa[0][1]+51200); s16x4 Wh6_1=TR(wa[0][1]+51712); SB;  SB;
    o5=__builtin_amdgcn_mfma_f32_32x32x16_bf16(CAT(Wl5_13,Wh5_13),JF(13),o5,0,0,0); OPQ(o5); s16x4 Wl6_2=TR(wa[0][1]+53248); s16x4 Wh6_2=TR(wa[0][1]+53760); SB;  SB;
    o5=__builtin_amdgcn_mfma_f32_32x32x16_bf16(CAT(Wl5_14,Wh5_14),JF(14),o5,0,0,0); OPQ(o5); s16x4 Wl6_3=TR(wa[0][1]+55296); s16x4 Wh6_3=TR(wa[0][1]+55808); SB;  SB;
    o5=__builtin_amdgcn_mfma_f32_32x32x16_bf16(CAT(Wl5_15,Wh5_15),JF(15),o5,0,0,0); OPQ(o5); s16x4 Wl6_4=TR(wa[0][1]+57344); s16x4 Wh6_4=TR(wa[0][1]+57856); SB;  SB;
        f32x16 o6=__builtin_amdgcn_mfma_f32_32x32x16_bf16(CAT(Wl6_0,Wh6_0),JF(0),z16,0,0,0); OPQ(o6); s16x4 Wl6_5=TR(wa[0][1]+59392); s16x4 Wh6_5=TR(wa[0][1]+59904); SB;  SB;
    o6=__builtin_amdgcn_mfma_f32_32x32x16_bf16(CAT(Wl6_1,Wh6_1),JF(1),o6,0,0,0); OPQ(o6); s16x4 Wl6_6=TR(wa[0][1]+61440); s16x4 Wh6_6=TR(wa[0][1]+61952); SB; unsigned pk5_0=pk2x2(o5[0],o5[1]); OPQ(pk5_0); unsigned pk5_1=pk2x2(o5[2],o5[3]); OPQ(pk5_1); SB;
    o6=__builtin_amdgcn_mfma_f32_32x32x16_bf16(CAT(Wl6_2,Wh6_2),JF(2),o6,0,0,0); OPQ(o6); s16x4 Wl6_7=TR(wa[0][1]+63488); s16x4 Wh6_7=TR(wa[0][1]+64000); SB; unsigned pk5_2=pk2x2(o5[4],o5[5]); OPQ(pk5_2); unsigned pk5_3=pk2x2(o5[6],o5[7]); OPQ(pk5_3); SB;
    o6=__builtin_amdgcn_mfma_f32_32x32x16_bf16(CAT(Wl6_3,Wh6_3),JF(3),o6,0,0,0); OPQ(o6); s16x4 Wl6_8=TR(wa[0][1]+0); s16x4 Wh6_8=TR(wa[0][1]+512); SB; unsigned pk5_4=pk2x2(o5[8],o5[9]); OPQ(pk5_4); unsigned pk5_5=pk2x2(o5[10],o5[11]); OPQ(pk5_5); unsigned pk5_6=pk2x2(o5[12],o5[13]); OPQ(pk5_6); SB;
    o6=__builtin_amdgcn_mfma_f32_32x32x16_bf16(CAT(Wl6_4,Wh6_4),JF(4),o6,0,0,0); OPQ(o6); s16x4 Wl6_9=TR(wa[0][1]+2048); s16x4 Wh6_9=TR(wa[0][1]+2560); SB; unsigned pk5_7=pk2x2(o5[14],o5[15]); OPQ(pk5_7); u32x4 Y2_0,Y2_1,Y2_2,Y2_3,Z2_0,Z2_1,Z2_2,Z2_3; SB;
    o6=__builtin_amdgcn_mfma_f32_32x32x16_bf16(CAT(Wl6_5,Wh6_5),JF(5),o6,0,0,0); OPQ(o6); s16x4 Wl6_10=TR(wa[0][1]+4096); s16x4 Wh6_10=TR(wa[0][1]+4608); SB;  SB;
    o6=__builtin_amdgcn_mfma_f32_32x32x16_bf16(CAT(Wl6_6,Wh6_6),JF(6),o6,0,0,0); OPQ(o6); s16x4 Wl6_11=TR(wa[0][1]+6144); s16x4 Wh6_11=TR(wa[0][1]+6656); SB; Y2_0=qsel<0x55555555u,0xB1>((u32x4){pk4_4,pk4_5,pk4_6,pk4_7},(u32x4){pk4_0,pk4_1,pk4_2,pk4_3}); SB;
    o6=__builtin_amdgcn_mfma_f32_32x32x16_bf16(CAT(Wl6_7,Wh6_7),JF(7),o6,0,0,0); OPQ(o6); s16x4 Wl6_12=TR(wa[0][1]+8192); s16x4 Wh6_12=TR(wa[0][1]+8704); SB; Y2_1=qsel<0xAAAAAAAAu,0xB1>((u32x4){pk4_0,pk4_1,pk4_2,pk4_3},(u32x4){pk4_4,pk4_5,pk4_6,pk4_7}); SB;
    o6=__builtin_amdgcn_mfma_f32_32x32x16_bf16(CAT(Wl6_8,Wh6_8),JF(8),o6,0,0,0); OPQ(o6); s16x4 Wl6_13=TR(wa[0][1]+10240); s16x4 Wh6_13=TR(wa[0][1]+10752); SB;  SB;
    o6=__builtin_amdgcn_mfma_f32_32x32x16_bf16(CAT(Wl6_9,Wh6_9),JF(9),o6,0,0,0); OPQ(o6); s16x4 Wl6_14=TR(wa[0][1]+12288); s16x4 Wh6_14=TR(wa[0][1]+12800); SB; Y2_2=qsel<0x55555555u,0xB1>((u32x4){pk5_4,pk5_5,pk5_6,pk5_7},(u32x4){pk5_0,pk5_1,pk5_2,pk5_3}); SB;
    o6=__builtin_amdgcn_mfma_f32_32x32x16_bf16(CAT(Wl6_10,Wh6_10),JF(10),o6,0,0,0); OPQ(o6); s16x4 Wl6_15=TR(wa[0][1]+14336); s16x4 Wh6_15=TR(wa[0][1]+14848); SB;  SB;
    o6=__builtin_amdgcn_mfma_f32_32x32x16_bf16(CAT(Wl6_11,Wh6_11),JF(11),o6,0,0,0); OPQ(o6); s16x4 Wl7_0=TR(wa[1][1]+49152); s16x4 Wh7_0=TR(wa[1][1]+49664); SB; Y2_3=qsel<0xAAAAAAAAu,0xB1>((u32x4){pk5_0,pk5_1,pk5_2,pk5_3},(u32x4){pk5_4,pk5_5,pk5_6,pk5_7}); SB;
    o6=__builtin_amdgcn_mfma_f32_32x32x16_bf16(CAT(Wl6_12,Wh6_12),JF(12),o6,0,0,0); OPQ(o6); s16x4 Wl7_1=TR(wa[1][1]+51200); s16x4 Wh7_1=TR(wa[1][1]+51712); SB; Z2_0=qsel<0x33333333u,0x4E>(Y2_2,Y2_0); SB;
    o6=__builtin_amdgcn_mfma_f32_32x32x16_bf16(CAT(Wl6_13,Wh6_13),JF(13),o6,0,0,0); OPQ(o6); s16x4 Wl7_2=TR(wa[1][1]+53248); s16x4 Wh7_2=TR(wa[1][1]+53760); SB; OSTORE(Z2_0,vo+256,0); SB;
    o6=__builtin_amdgcn_mfma_f32_32x32x16_bf16(CAT(Wl6_14,Wh6_14),JF(14),o6,0,0,0); OPQ(o6); s16x4 Wl7_3=TR(wa[1][1]+55296); s16x4 Wh7_3=TR(wa[1][1]+55808); SB; Z2_1=qsel<0x33333333u,0x4E>(Y2_3,Y2_1); SB;
    o6=__builtin_amdgcn_mfma_f32_32x32x16_bf16(CAT(Wl6_15,Wh6_15),JF(15),o6,0,0,0); OPQ(o6); s16x4 Wl7_4=TR(wa[1][1]+57344); s16x4 Wh7_4=TR(wa[1][1]+57856); SB; OSTORE(Z2_1,vo+256,4096); SB;
    f32x16 o7=__builtin_amdgcn_mfma_f32_32x32x16_bf16(CAT(Wl7_0,Wh7_0),JF(0),z16,0,0,0); OPQ(o7); s16x4 Wl7_5=TR(wa[1][1]+59392); s16x4 Wh7_5=TR(wa[1][1]+59904); SB; Z2_2=qsel<0xCCCCCCCCu,0x4E>(Y2_0,Y2_2); SB;
    o7=__builtin_amdgcn_mfma_f32_32x32x16_bf16(CAT(Wl7_1,Wh7_1),JF(1),o7,0,0,0); OPQ(o7); s16x4 Wl7_6=TR(wa[1][1]+61440); s16x4 Wh7_6=TR(wa[1][1]+61952); SB; OSTORE(Z2_2,vo+256,8192); SB;
    o7=__builtin_amdgcn_mfma_f32_32x32x16_bf16(CAT(Wl7_2,Wh7_2),JF(2),o7,0,0,0); OPQ(o7); s16x4 Wl7_7=TR(wa[1][1]+63488); s16x4 Wh7_7=TR(wa[1][1]+64000); SB; Z2_3=qsel<0xCCCCCCCCu,0x4E>(Y2_1,Y2_3); SB;
    o7=__builtin_amdgcn_mfma_f32_32x32x16_bf16(CAT(Wl7_3,Wh7_3),JF(3),o7,0,0,0); OPQ(o7); s16x4 Wl7_8=TR(wa[1][1]+0); s16x4 Wh7_8=TR(wa[1][1]+512); SB; OSTORE(Z2_3,vo+256,12288); SB;
    o7=__builtin_amdgcn_mfma_f32_32x32x16_bf16(CAT(Wl7_4,Wh7_4),JF(4),o7,0,0,0); OPQ(o7); s16x4 Wl7_9=TR(wa[1][1]+2048); s16x4 Wh7_9=TR(wa[1][1]+2560); SB; unsigned pk6_0=pk2x2(o6[0],o6[1]); OPQ(pk6_0); SB;
    o7=__builtin_amdgcn_mfma_f32_32x32x16_bf16(CAT(Wl7_5,Wh7_5),JF(5),o7,0,0,0); OPQ(o7); s16x4 Wl7_10=TR(wa[1][1]+4096); s16x4 Wh7_10=TR(wa[1][1]+4608); SB; unsigned pk6_1=pk2x2(o6[2],o6[3]); OPQ(pk6_1); unsigned pk6_2=pk2x2(o6[4],o6[5]); OPQ(pk6_2); unsigned pk6_3=pk2x2(o6[6],o6[7]); OPQ(pk6_3); SB;
    o7=__builtin_amdgcn_mfma_f32_32x32x16_bf16(CAT(Wl7_6,Wh7_6),JF(6),o7,0,0,0); OPQ(o7); s16x4 Wl7_11=TR(wa[1][1]+6144); s16x4 Wh7_11=TR(wa[1][1]+6656); SB; unsigned pk6_4=pk2x2(o6[8],o6[9]); OPQ(pk6_4); unsigned pk6_5=pk2x2(o6[10],o6[11]); OPQ(pk6_5); SB;
    o7=__builtin_amdgcn_mfma_f32_32x32x16_bf16(CAT(Wl7_7,Wh7_7),JF(7),o7,0,0,0); OPQ(o7); s16x4 Wl7_12=TR(wa[1][1]+8192); s16x4 Wh7_12=TR(wa[1][1]+8704); SB; unsigned pk6_6=pk2x2(o6[12],o6[13]); OPQ(pk6_6); unsigned pk6_7=pk2x2(o6[14],o6[15]); OPQ(pk6_7); SB;
    o7=__builtin_amdgcn_mfma_f32_32x32x16_bf16(CAT(Wl7_8,Wh7_8),JF(8),o7,0,0,0); OPQ(o7); s16x4 Wl7_13=TR(wa[1][1]+10240); s16x4 Wh7_13=TR(wa[1][1]+10752); SB;  SB;
    o7=__builtin_amdgcn_mfma_f32_32x32x16_bf16(CAT(Wl7_9,Wh7_9),JF(9),o7,0,0,0); OPQ(o7); s16x4 Wl7_14=TR(wa[1][1]+12288); s16x4 Wh7_14=TR(wa[1][1]+12800); SB;  SB;
    o7=__builtin_amdgcn_mfma_f32_32x32x16_bf16(CAT(Wl7_10,Wh7_10),JF(10),o7,0,0,0); OPQ(o7); s16x4 Wl7_15=TR(wa[1][1]+14336); s16x4 Wh7_15=TR(wa[1][1]+14848); SB;  SB;
    o7=__builtin_amdgcn_mfma_f32_32x32x16_bf16(CAT(Wl7_11,Wh7_11),JF(11),o7,0,0,0); OPQ(o7);  SB;  SB;
    o7=__builtin_amdgcn_mfma_f32_32x32x16_bf16(CAT(Wl7_12,Wh7_12),JF(12),o7,0,0,0); OPQ(o7);  SB;  SB;
    o7=__builtin_amdgcn_mfma_f32_32x32x16_bf16(CAT(Wl7_13,Wh7_13),JF(13),o7,0,0,0); OPQ(o7);  SB;  SB;
    o7=__builtin_amdgcn_mfma_f32_32x32x16_bf16(CAT(Wl7_14,Wh7_14),JF(14),o7,0,0,0); OPQ(o7);  SB;  SB;
    o7=__builtin_amdgcn_mfma_f32_32x32x16_bf16(CAT(Wl7_15,Wh7_15),JF(15),o7,0,0,0); OPQ(o7);  SB;  SB;
    unsigned pk7_0=pk2x2(o7[0],o7[1]); OPQ(pk7_0); SB;
    unsigned pk7_1=pk2x2(o7[2],o7[3]); OPQ(pk7_1); SB;
    unsigned pk7_2=pk2x2(o7[4],o7[5]); OPQ(pk7_2); SB;
    unsigned pk7_3=pk2x2(o7[6],o7[7]); OPQ(pk7_3); SB;
    unsigned pk7_4=pk2x2(o7[8],o7[9]); OPQ(pk7_4); SB;
    unsigned pk7_5=pk2x2(o7[10],o7[11]); OPQ(pk7_5); SB;
    unsigned pk7_6=pk2x2(o7[12],o7[13]); OPQ(pk7_6); SB;
    unsigned pk7_7=pk2x2(o7[14],o7[15]); OPQ(pk7_7); SB;
    u32x4 Y3_0,Y3_1,Y3_2,Y3_3,Z3_0,Z3_1,Z3_2,Z3_3; SB;
    Y3_0=qsel<0x55555555u,0xB1>((u32x4){pk6_4,pk6_5,pk6_6,pk6_7},(u32x4){pk6_0,pk6_1,pk6_2,pk6_3}); SB;
    Y3_1=qsel<0xAAAAAAAAu,0xB1>((u32x4){pk6_0,pk6_1,pk6_2,pk6_3},(u32x4){pk6_4,pk6_5,pk6_6,pk6_7}); SB;
    Y3_2=qsel<0x55555555u,0xB1>((u32x4){pk7_4,pk7_5,pk7_6,pk7_7},(u32x4){pk7_0,pk7_1,pk7_2,pk7_3}); SB;
    Y3_3=qsel<0xAAAAAAAAu,0xB1>((u32x4){pk7_0,pk7_1,pk7_2,pk7_3},(u32x4){pk7_4,pk7_5,pk7_6,pk7_7}); SB;
    Z3_0=qsel<0x33333333u,0x4E>(Y3_2,Y3_0); SB;
    OSTORE(Z3_0,vo+384,0); SB;
    Z3_1=qsel<0x33333333u,0x4E>(Y3_3,Y3_1); SB;
    OSTORE(Z3_1,vo+384,4096); SB;
    Z3_2=qsel<0xCCCCCCCCu,0x4E>(Y3_0,Y3_2); SB;
    OSTORE(Z3_2,vo+384,8192); SB;
    Z3_3=qsel<0xCCCCCCCCu,0x4E>(Y3_1,Y3_3); SB;
    OSTORE(Z3_3,vo+384,12288); SB;
     asm volatile(".Lend2:");
  }
}

extern "C" void kernel_launch(void* const* d_in, const int* in_sizes, int n_in, void* d_out, int out_size, void* d_ws, size_t ws_size, hipStream_t stream) {
  (void)hipFuncSetAttribute((const void*)fused_kernel, hipFuncAttributeMaxDynamicSharedMemorySize, 163840);
  fused_kernel<<<HELPERS?256:251,512,163840,stream>>>((const char*)d_in[0],(const char*)d_in[1],(const char*)d_in[2],(const char*)d_in[3],(const char*)d_in[4],(const char*)d_in[5],(const char*)d_in[6],(char*)d_out,(char*)d_ws);
}
```
